```python
import torch

def _softmax(x, axis=-1):
    return torch.softmax(x, dim=axis)
def _gelu(x):
    return torch.nn.functional.gelu(x, approximate="tanh")
def _silu(x):
    return torch.nn.functional.silu(x)
def _sigmoid(x):
    return torch.sigmoid(x)
def _layernorm(x, axis=-1, eps=1e-5):
    m = x.mean(dim=axis, keepdim=True)
    v = x.var(dim=axis, keepdim=True, unbiased=False)
    return (x - m) / torch.sqrt(v + eps)
def _scatter_add(tgt, idx, src, axis=0):
    return tgt.clone().index_add_(axis, idx, src)
def _segment_sum(src, seg, n):
    out = torch.zeros((n,) + src.shape[1:], dtype=src.dtype)
    return out.index_add_(0, seg, src)
def _pad(x, pw):
    flat = tuple(v for pair in reversed(pw) for v in pair)
    return torch.nn.functional.pad(x, flat)

kernel_name = 'sequential_preLN__spectral_gfnet__moe_arch_mod__prompt'

def setup_inputs():

    _w = (lambda a: (lambda t: t.to(torch.bfloat16) if t.is_floating_point() else t)(torch.as_tensor(a)))
    def _A_setup():
        B, S, D = 1, 2048, 576
        Df = D // 2 + 1
        return {
            "x": _w((lambda *s: torch.randn(*s, dtype=torch.bfloat16) * 0.05)(B, S, D)),
            "w_r": _w((lambda *s: torch.randn(*s, dtype=torch.bfloat16) * 0.8)(S, Df) * 0.02),
            "w_i": _w((lambda *s: torch.randn(*s, dtype=torch.bfloat16) * 0.8)(S, Df) * 0.02),
        }
    _A_in = _A_setup()

    def _B_setup():
        B, S, D, F, C = 1, 2048, 576, 4608, 8
        return {
            "x": _w((lambda *s: torch.randn(*s, dtype=torch.bfloat16) * 0.05)(B, S, D)),
            "w_r": _w((lambda *s: torch.randn(*s, dtype=torch.bfloat16) * 0.2)(D)),
            "w_gate": _w((lambda *s: torch.randn(*s, dtype=torch.bfloat16) * 0.2)(D, F)),
            "w_up": _w((lambda *s: torch.randn(*s, dtype=torch.bfloat16) * 0.2)(D, F)),
            "w_down": _w((lambda *s: torch.randn(*s, dtype=torch.bfloat16) * 0.2)(F, D)),
        }
    _B_in = _B_setup()

    return {
        "A_x": _A_in["x"],
        "A_w_r": _A_in["w_r"],
        "A_w_i": _A_in["w_i"],
        "B_x": _B_in["x"],
        "B_w_r": _B_in["w_r"],
        "B_w_gate": _B_in["w_gate"],
        "B_w_up": _B_in["w_up"],
        "B_w_down": _B_in["w_down"],
    }

def reference(A_x, A_w_r, A_w_i, B_x, B_w_r, B_w_gate, B_w_up, B_w_down):
    def _A_ref(x, w_r, w_i):
        B, S, D = x.shape
        X = (lambda x, axes=(-2,-1): torch.fft.rfft2(x.float(), dim=axes))(x, axes=(-2, -1))
        W = w_r + 1j * w_i
        X = X * W
        y = (lambda x, s, axes=(-2,-1): torch.fft.irfft2(x, s=s, dim=axes).to(torch.bfloat16))(X, (S, D), axes=(-2, -1))
        return y

    def _B_ref(x, w_r, w_gate, w_up, w_down):
        B, S, D = x.shape
        C = 8
        r = x @ w_r
        idx = torch.argsort(-r, axis=-1)[:, :C]
        idx_e = (lambda x, n, axis: torch.repeat_interleave(x, n, dim=axis))(idx.reshape(B, C, 1), D, axis=2)
        x_sel = (lambda x, i, axis: torch.take_along_dim(x, i, dim=axis))(x, idx_e, axis=1)
        g = _silu(x_sel @ w_gate)
        u = x_sel @ w_up
        y = (g * u) @ w_down
        r_sel = _sigmoid((lambda x, i, axis: torch.take_along_dim(x, i, dim=axis))(r, idx, axis=1))
        y = y * r_sel.reshape(B, C, 1)
        b_off = (torch.arange(B) * S).reshape(B, 1)
        flat_idx = (idx + b_off).reshape(B * C)
        zero = (x * 0.0).reshape(B * S, D)
        upd = _scatter_add(zero, flat_idx, y.reshape(B * C, D), axis=0)
        return x + upd.reshape(B, S, D)

    x = A_x
    h = x + _A_ref(_layernorm(x, axis=-1), A_w_r, A_w_i)
    h2 = h + _B_ref(_layernorm(h, axis=-1), B_w_r, B_w_gate, B_w_up, B_w_down)
    return h2
```

```cpp
#include <hip/hip_runtime.h>
#include <hip/hip_bf16.h>
#include <stdint.h>

__device__ __attribute__((aligned(16))) const float TT1[4096] = {
1.000000000e+00f,0.000000000e+00f,1.000000000e+00f,0.000000000e+00f,1.000000000e+00f,0.000000000e+00f,9.999952938e-01f,-3.067956763e-03f,
1.000000000e+00f,0.000000000e+00f,9.999811753e-01f,-6.135884649e-03f,1.000000000e+00f,0.000000000e+00f,9.999576446e-01f,-9.203754782e-03f,
1.000000000e+00f,0.000000000e+00f,9.999247018e-01f,-1.227153829e-02f,1.000000000e+00f,0.000000000e+00f,9.998823475e-01f,-1.533920628e-02f,
1.000000000e+00f,0.000000000e+00f,9.998305818e-01f,-1.840672991e-02f,1.000000000e+00f,0.000000000e+00f,9.997694054e-01f,-2.147408028e-02f,
1.000000000e+00f,0.000000000e+00f,9.996988187e-01f,-2.454122852e-02f,1.000000000e+00f,0.000000000e+00f,9.996188225e-01f,-2.760814578e-02f,
1.000000000e+00f,0.000000000e+00f,9.995294175e-01f,-3.067480318e-02f,1.000000000e+00f,0.000000000e+00f,9.994306046e-01f,-3.374117185e-02f,
1.000000000e+00f,0.000000000e+00f,9.993223846e-01f,-3.680722294e-02f,1.000000000e+00f,0.000000000e+00f,9.992047586e-01f,-3.987292759e-02f,
1.000000000e+00f,0.000000000e+00f,9.990777278e-01f,-4.293825693e-02f,1.000000000e+00f,0.000000000e+00f,9.989412932e-01f,-4.600318213e-02f,
1.000000000e+00f,0.000000000e+00f,9.987954562e-01f,-4.906767433e-02f,1.000000000e+00f,0.000000000e+00f,9.986402182e-01f,-5.213170468e-02f,
1.000000000e+00f,0.000000000e+00f,9.984755806e-01f,-5.519524435e-02f,1.000000000e+00f,0.000000000e+00f,9.983015449e-01f,-5.825826450e-02f,
1.000000000e+00f,0.000000000e+00f,9.981181129e-01f,-6.132073630e-02f,1.000000000e+00f,0.000000000e+00f,9.979252862e-01f,-6.438263093e-02f,
1.000000000e+00f,0.000000000e+00f,9.977230666e-01f,-6.744391956e-02f,1.000000000e+00f,0.000000000e+00f,9.975114561e-01f,-7.050457339e-02f,
1.000000000e+00f,0.000000000e+00f,9.972904567e-01f,-7.356456360e-02f,1.000000000e+00f,0.000000000e+00f,9.970600703e-01f,-7.662386139e-02f,
1.000000000e+00f,0.000000000e+00f,9.968202993e-01f,-7.968243797e-02f,1.000000000e+00f,0.000000000e+00f,9.965711458e-01f,-8.274026455e-02f,
1.000000000e+00f,0.000000000e+00f,9.963126122e-01f,-8.579731234e-02f,1.000000000e+00f,0.000000000e+00f,9.960447009e-01f,-8.885355258e-02f,
1.000000000e+00f,0.000000000e+00f,9.957674145e-01f,-9.190895650e-02f,1.000000000e+00f,0.000000000e+00f,9.954807555e-01f,-9.496349533e-02f,
1.000000000e+00f,0.000000000e+00f,9.951847267e-01f,-9.801714033e-02f,1.000000000e+00f,0.000000000e+00f,9.948793308e-01f,-1.010698628e-01f,
1.000000000e+00f,0.000000000e+00f,9.945645707e-01f,-1.041216339e-01f,1.000000000e+00f,0.000000000e+00f,9.942404495e-01f,-1.071724250e-01f,
1.000000000e+00f,0.000000000e+00f,9.939069700e-01f,-1.102222073e-01f,1.000000000e+00f,0.000000000e+00f,9.935641355e-01f,-1.132709522e-01f,
1.000000000e+00f,0.000000000e+00f,9.932119492e-01f,-1.163186309e-01f,1.000000000e+00f,0.000000000e+00f,9.928504145e-01f,-1.193652148e-01f,
1.000000000e+00f,0.000000000e+00f,9.924795346e-01f,-1.224106752e-01f,1.000000000e+00f,0.000000000e+00f,9.920993131e-01f,-1.254549834e-01f,
1.000000000e+00f,0.000000000e+00f,9.917097537e-01f,-1.284981108e-01f,1.000000000e+00f,0.000000000e+00f,9.913108598e-01f,-1.315400287e-01f,
1.000000000e+00f,0.000000000e+00f,9.909026354e-01f,-1.345807085e-01f,1.000000000e+00f,0.000000000e+00f,9.904850843e-01f,-1.376201216e-01f,
1.000000000e+00f,0.000000000e+00f,9.900582103e-01f,-1.406582393e-01f,1.000000000e+00f,0.000000000e+00f,9.896220175e-01f,-1.436950332e-01f,
1.000000000e+00f,0.000000000e+00f,9.891765100e-01f,-1.467304745e-01f,1.000000000e+00f,0.000000000e+00f,9.887216920e-01f,-1.497645347e-01f,
1.000000000e+00f,0.000000000e+00f,9.882575677e-01f,-1.527971853e-01f,1.000000000e+00f,0.000000000e+00f,9.877841416e-01f,-1.558283977e-01f,
1.000000000e+00f,0.000000000e+00f,9.873014182e-01f,-1.588581433e-01f,1.000000000e+00f,0.000000000e+00f,9.868094018e-01f,-1.618863938e-01f,
1.000000000e+00f,0.000000000e+00f,9.863080972e-01f,-1.649131205e-01f,1.000000000e+00f,0.000000000e+00f,9.857975092e-01f,-1.679382950e-01f,
1.000000000e+00f,0.000000000e+00f,9.852776424e-01f,-1.709618888e-01f,1.000000000e+00f,0.000000000e+00f,9.847485018e-01f,-1.739838734e-01f,
1.000000000e+00f,0.000000000e+00f,9.842100924e-01f,-1.770042204e-01f,1.000000000e+00f,0.000000000e+00f,9.836624192e-01f,-1.800229014e-01f,
1.000000000e+00f,0.000000000e+00f,9.831054874e-01f,-1.830398880e-01f,1.000000000e+00f,0.000000000e+00f,9.825393023e-01f,-1.860551517e-01f,
1.000000000e+00f,0.000000000e+00f,9.819638691e-01f,-1.890686641e-01f,1.000000000e+00f,0.000000000e+00f,9.813791933e-01f,-1.920803970e-01f,
1.000000000e+00f,0.000000000e+00f,9.807852804e-01f,-1.950903220e-01f,1.000000000e+00f,0.000000000e+00f,9.801821360e-01f,-1.980984107e-01f,
1.000000000e+00f,0.000000000e+00f,9.795697657e-01f,-2.011046348e-01f,1.000000000e+00f,0.000000000e+00f,9.789481753e-01f,-2.041089661e-01f,
1.000000000e+00f,0.000000000e+00f,9.783173707e-01f,-2.071113762e-01f,1.000000000e+00f,0.000000000e+00f,9.776773578e-01f,-2.101118369e-01f,
1.000000000e+00f,0.000000000e+00f,9.770281427e-01f,-2.131103199e-01f,1.000000000e+00f,0.000000000e+00f,9.763697313e-01f,-2.161067971e-01f,
1.000000000e+00f,0.000000000e+00f,9.757021300e-01f,-2.191012402e-01f,1.000000000e+00f,0.000000000e+00f,9.750253451e-01f,-2.220936210e-01f,
1.000000000e+00f,0.000000000e+00f,9.743393828e-01f,-2.250839114e-01f,1.000000000e+00f,0.000000000e+00f,9.736442497e-01f,-2.280720832e-01f,
1.000000000e+00f,0.000000000e+00f,9.729399522e-01f,-2.310581083e-01f,1.000000000e+00f,0.000000000e+00f,9.722264971e-01f,-2.340419586e-01f,
1.000000000e+00f,0.000000000e+00f,9.715038910e-01f,-2.370236060e-01f,1.000000000e+00f,0.000000000e+00f,9.707721407e-01f,-2.400030224e-01f,
1.000000000e+00f,0.000000000e+00f,9.700312532e-01f,-2.429801799e-01f,1.000000000e+00f,0.000000000e+00f,9.692812354e-01f,-2.459550503e-01f,
1.000000000e+00f,0.000000000e+00f,9.685220943e-01f,-2.489276057e-01f,1.000000000e+00f,0.000000000e+00f,9.677538371e-01f,-2.518978182e-01f,
1.000000000e+00f,0.000000000e+00f,9.669764710e-01f,-2.548656596e-01f,1.000000000e+00f,0.000000000e+00f,9.661900034e-01f,-2.578311022e-01f,
1.000000000e+00f,0.000000000e+00f,9.653944417e-01f,-2.607941179e-01f,1.000000000e+00f,0.000000000e+00f,9.645897933e-01f,-2.637546790e-01f,
1.000000000e+00f,0.000000000e+00f,9.637760658e-01f,-2.667127575e-01f,1.000000000e+00f,0.000000000e+00f,9.629532669e-01f,-2.696683256e-01f,
1.000000000e+00f,0.000000000e+00f,9.621214043e-01f,-2.726213554e-01f,1.000000000e+00f,0.000000000e+00f,9.612804858e-01f,-2.755718193e-01f,
1.000000000e+00f,0.000000000e+00f,9.604305194e-01f,-2.785196894e-01f,1.000000000e+00f,0.000000000e+00f,9.595715131e-01f,-2.814649379e-01f,
1.000000000e+00f,0.000000000e+00f,9.587034749e-01f,-2.844075372e-01f,1.000000000e+00f,0.000000000e+00f,9.578264130e-01f,-2.873474595e-01f,
1.000000000e+00f,0.000000000e+00f,9.569403357e-01f,-2.902846773e-01f,1.000000000e+00f,0.000000000e+00f,9.560452513e-01f,-2.932191627e-01f,
1.000000000e+00f,0.000000000e+00f,9.551411683e-01f,-2.961508882e-01f,1.000000000e+00f,0.000000000e+00f,9.542280951e-01f,-2.990798263e-01f,
1.000000000e+00f,0.000000000e+00f,9.533060404e-01f,-3.020059493e-01f,1.000000000e+00f,0.000000000e+00f,9.523750127e-01f,-3.049292297e-01f,
1.000000000e+00f,0.000000000e+00f,9.514350210e-01f,-3.078496400e-01f,1.000000000e+00f,0.000000000e+00f,9.504860739e-01f,-3.107671527e-01f,
1.000000000e+00f,0.000000000e+00f,9.495281806e-01f,-3.136817404e-01f,1.000000000e+00f,0.000000000e+00f,9.485613499e-01f,-3.165933756e-01f,
1.000000000e+00f,0.000000000e+00f,9.475855910e-01f,-3.195020308e-01f,1.000000000e+00f,0.000000000e+00f,9.466009131e-01f,-3.224076788e-01f,
1.000000000e+00f,0.000000000e+00f,9.456073254e-01f,-3.253102922e-01f,1.000000000e+00f,0.000000000e+00f,9.446048373e-01f,-3.282098436e-01f,
1.000000000e+00f,0.000000000e+00f,9.435934582e-01f,-3.311063058e-01f,1.000000000e+00f,0.000000000e+00f,9.425731976e-01f,-3.339996514e-01f,
1.000000000e+00f,0.000000000e+00f,9.415440652e-01f,-3.368898534e-01f,1.000000000e+00f,0.000000000e+00f,9.405060706e-01f,-3.397768844e-01f,
1.000000000e+00f,0.000000000e+00f,9.394592236e-01f,-3.426607173e-01f,1.000000000e+00f,0.000000000e+00f,9.384035341e-01f,-3.455413250e-01f,
1.000000000e+00f,0.000000000e+00f,9.373390119e-01f,-3.484186802e-01f,1.000000000e+00f,0.000000000e+00f,9.362656672e-01f,-3.512927561e-01f,
1.000000000e+00f,0.000000000e+00f,9.351835099e-01f,-3.541635254e-01f,1.000000000e+00f,0.000000000e+00f,9.340925504e-01f,-3.570309612e-01f,
1.000000000e+00f,0.000000000e+00f,9.329927988e-01f,-3.598950365e-01f,1.000000000e+00f,0.000000000e+00f,9.318842656e-01f,-3.627557244e-01f,
1.000000000e+00f,0.000000000e+00f,9.307669611e-01f,-3.656129978e-01f,1.000000000e+00f,0.000000000e+00f,9.296408958e-01f,-3.684668300e-01f,
1.000000000e+00f,0.000000000e+00f,9.285060805e-01f,-3.713171940e-01f,1.000000000e+00f,0.000000000e+00f,9.273625257e-01f,-3.741640630e-01f,
1.000000000e+00f,0.000000000e+00f,9.262102421e-01f,-3.770074102e-01f,1.000000000e+00f,0.000000000e+00f,9.250492408e-01f,-3.798472089e-01f,
1.000000000e+00f,0.000000000e+00f,9.238795325e-01f,-3.826834324e-01f,1.000000000e+00f,0.000000000e+00f,9.227011283e-01f,-3.855160538e-01f,
1.000000000e+00f,0.000000000e+00f,9.215140393e-01f,-3.883450467e-01f,1.000000000e+00f,0.000000000e+00f,9.203182767e-01f,-3.911703843e-01f,
1.000000000e+00f,0.000000000e+00f,9.191138517e-01f,-3.939920401e-01f,1.000000000e+00f,0.000000000e+00f,9.179007756e-01f,-3.968099874e-01f,
1.000000000e+00f,0.000000000e+00f,9.166790599e-01f,-3.996241998e-01f,1.000000000e+00f,0.000000000e+00f,9.154487161e-01f,-4.024346509e-01f,
1.000000000e+00f,0.000000000e+00f,9.142097557e-01f,-4.052413140e-01f,1.000000000e+00f,0.000000000e+00f,9.129621904e-01f,-4.080441629e-01f,
1.000000000e+00f,0.000000000e+00f,9.117060320e-01f,-4.108431711e-01f,1.000000000e+00f,0.000000000e+00f,9.104412923e-01f,-4.136383122e-01f,
1.000000000e+00f,0.000000000e+00f,9.091679831e-01f,-4.164295601e-01f,1.000000000e+00f,0.000000000e+00f,9.078861165e-01f,-4.192168884e-01f,
1.000000000e+00f,0.000000000e+00f,9.065957045e-01f,-4.220002708e-01f,1.000000000e+00f,0.000000000e+00f,9.052967593e-01f,-4.247796812e-01f,
1.000000000e+00f,0.000000000e+00f,9.039892931e-01f,-4.275550934e-01f,1.000000000e+00f,0.000000000e+00f,9.026733182e-01f,-4.303264813e-01f,
1.000000000e+00f,0.000000000e+00f,9.013488470e-01f,-4.330938189e-01f,1.000000000e+00f,0.000000000e+00f,9.000158920e-01f,-4.358570799e-01f,
1.000000000e+00f,0.000000000e+00f,8.986744657e-01f,-4.386162385e-01f,1.000000000e+00f,0.000000000e+00f,8.973245807e-01f,-4.413712687e-01f,
1.000000000e+00f,0.000000000e+00f,8.959662498e-01f,-4.441221446e-01f,1.000000000e+00f,0.000000000e+00f,8.945994856e-01f,-4.468688402e-01f,
1.000000000e+00f,0.000000000e+00f,8.932243012e-01f,-4.496113297e-01f,1.000000000e+00f,0.000000000e+00f,8.918407094e-01f,-4.523495872e-01f,
1.000000000e+00f,0.000000000e+00f,8.904487232e-01f,-4.550835871e-01f,1.000000000e+00f,0.000000000e+00f,8.890483559e-01f,-4.578133036e-01f,
1.000000000e+00f,0.000000000e+00f,8.876396204e-01f,-4.605387110e-01f,1.000000000e+00f,0.000000000e+00f,8.862225301e-01f,-4.632597836e-01f,
1.000000000e+00f,0.000000000e+00f,8.847970984e-01f,-4.659764958e-01f,1.000000000e+00f,0.000000000e+00f,8.833633387e-01f,-4.686888220e-01f,
1.000000000e+00f,0.000000000e+00f,8.819212643e-01f,-4.713967368e-01f,1.000000000e+00f,0.000000000e+00f,8.804708891e-01f,-4.741002147e-01f,
1.000000000e+00f,0.000000000e+00f,8.790122264e-01f,-4.767992301e-01f,1.000000000e+00f,0.000000000e+00f,8.775452902e-01f,-4.794937577e-01f,
1.000000000e+00f,0.000000000e+00f,8.760700942e-01f,-4.821837721e-01f,1.000000000e+00f,0.000000000e+00f,8.745866523e-01f,-4.848692480e-01f,
1.000000000e+00f,0.000000000e+00f,8.730949784e-01f,-4.875501601e-01f,1.000000000e+00f,0.000000000e+00f,8.715950867e-01f,-4.902264833e-01f,
1.000000000e+00f,0.000000000e+00f,8.700869911e-01f,-4.928981922e-01f,1.000000000e+00f,0.000000000e+00f,8.685707060e-01f,-4.955652618e-01f,
1.000000000e+00f,0.000000000e+00f,8.670462455e-01f,-4.982276670e-01f,1.000000000e+00f,0.000000000e+00f,8.655136241e-01f,-5.008853826e-01f,
1.000000000e+00f,0.000000000e+00f,8.639728561e-01f,-5.035383837e-01f,1.000000000e+00f,0.000000000e+00f,8.624239561e-01f,-5.061866453e-01f,
1.000000000e+00f,0.000000000e+00f,8.608669386e-01f,-5.088301425e-01f,1.000000000e+00f,0.000000000e+00f,8.593018184e-01f,-5.114688504e-01f,
1.000000000e+00f,0.000000000e+00f,8.577286100e-01f,-5.141027442e-01f,1.000000000e+00f,0.000000000e+00f,8.561473284e-01f,-5.167317990e-01f,
1.000000000e+00f,0.000000000e+00f,8.545579884e-01f,-5.193559902e-01f,1.000000000e+00f,0.000000000e+00f,8.529606049e-01f,-5.219752929e-01f,
1.000000000e+00f,0.000000000e+00f,8.513551931e-01f,-5.245896827e-01f,1.000000000e+00f,0.000000000e+00f,8.497417680e-01f,-5.271991348e-01f,
1.000000000e+00f,0.000000000e+00f,8.481203448e-01f,-5.298036247e-01f,1.000000000e+00f,0.000000000e+00f,8.464909388e-01f,-5.324031279e-01f,
1.000000000e+00f,0.000000000e+00f,8.448535652e-01f,-5.349976199e-01f,1.000000000e+00f,0.000000000e+00f,8.432082396e-01f,-5.375870763e-01f,
1.000000000e+00f,0.000000000e+00f,8.415549774e-01f,-5.401714727e-01f,1.000000000e+00f,0.000000000e+00f,8.398937942e-01f,-5.427507849e-01f,
1.000000000e+00f,0.000000000e+00f,8.382247056e-01f,-5.453249884e-01f,1.000000000e+00f,0.000000000e+00f,8.365477272e-01f,-5.478940592e-01f,
1.000000000e+00f,0.000000000e+00f,8.348628750e-01f,-5.504579729e-01f,1.000000000e+00f,0.000000000e+00f,8.331701647e-01f,-5.530167056e-01f,
1.000000000e+00f,0.000000000e+00f,8.314696123e-01f,-5.555702330e-01f,1.000000000e+00f,0.000000000e+00f,8.297612338e-01f,-5.581185312e-01f,
1.000000000e+00f,0.000000000e+00f,8.280450453e-01f,-5.606615762e-01f,1.000000000e+00f,0.000000000e+00f,8.263210628e-01f,-5.631993440e-01f,
1.000000000e+00f,0.000000000e+00f,8.245893028e-01f,-5.657318108e-01f,1.000000000e+00f,0.000000000e+00f,8.228497814e-01f,-5.682589527e-01f,
1.000000000e+00f,0.000000000e+00f,8.211025150e-01f,-5.707807459e-01f,1.000000000e+00f,0.000000000e+00f,8.193475201e-01f,-5.732971667e-01f,
1.000000000e+00f,0.000000000e+00f,8.175848132e-01f,-5.758081914e-01f,1.000000000e+00f,0.000000000e+00f,8.158144108e-01f,-5.783137964e-01f,
1.000000000e+00f,0.000000000e+00f,8.140363297e-01f,-5.808139581e-01f,1.000000000e+00f,0.000000000e+00f,8.122505866e-01f,-5.833086529e-01f,
1.000000000e+00f,0.000000000e+00f,8.104571983e-01f,-5.857978575e-01f,1.000000000e+00f,0.000000000e+00f,8.086561816e-01f,-5.882815482e-01f,
1.000000000e+00f,0.000000000e+00f,8.068475535e-01f,-5.907597019e-01f,1.000000000e+00f,0.000000000e+00f,8.050313311e-01f,-5.932322950e-01f,
1.000000000e+00f,0.000000000e+00f,8.032075315e-01f,-5.956993045e-01f,1.000000000e+00f,0.000000000e+00f,8.013761717e-01f,-5.981607070e-01f,
1.000000000e+00f,0.000000000e+00f,7.995372691e-01f,-6.006164794e-01f,1.000000000e+00f,0.000000000e+00f,7.976908409e-01f,-6.030665985e-01f,
1.000000000e+00f,0.000000000e+00f,7.958369046e-01f,-6.055110414e-01f,1.000000000e+00f,0.000000000e+00f,7.939754776e-01f,-6.079497850e-01f,
1.000000000e+00f,0.000000000e+00f,7.921065773e-01f,-6.103828063e-01f,1.000000000e+00f,0.000000000e+00f,7.902302214e-01f,-6.128100824e-01f,
1.000000000e+00f,0.000000000e+00f,7.883464276e-01f,-6.152315906e-01f,1.000000000e+00f,0.000000000e+00f,7.864552136e-01f,-6.176473079e-01f,
1.000000000e+00f,0.000000000e+00f,7.845565972e-01f,-6.200572118e-01f,1.000000000e+00f,0.000000000e+00f,7.826505962e-01f,-6.224612794e-01f,
1.000000000e+00f,0.000000000e+00f,7.807372286e-01f,-6.248594881e-01f,1.000000000e+00f,0.000000000e+00f,7.788165124e-01f,-6.272518155e-01f,
1.000000000e+00f,0.000000000e+00f,7.768884657e-01f,-6.296382389e-01f,1.000000000e+00f,0.000000000e+00f,7.749531066e-01f,-6.320187359e-01f,
1.000000000e+00f,0.000000000e+00f,7.730104534e-01f,-6.343932842e-01f,1.000000000e+00f,0.000000000e+00f,7.710605243e-01f,-6.367618612e-01f,
1.000000000e+00f,0.000000000e+00f,7.691033376e-01f,-6.391244449e-01f,1.000000000e+00f,0.000000000e+00f,7.671389119e-01f,-6.414810128e-01f,
1.000000000e+00f,0.000000000e+00f,7.651672656e-01f,-6.438315429e-01f,1.000000000e+00f,0.000000000e+00f,7.631884173e-01f,-6.461760130e-01f,
1.000000000e+00f,0.000000000e+00f,7.612023855e-01f,-6.485144010e-01f,1.000000000e+00f,0.000000000e+00f,7.592091890e-01f,-6.508466850e-01f,
1.000000000e+00f,0.000000000e+00f,7.572088465e-01f,-6.531728430e-01f,1.000000000e+00f,0.000000000e+00f,7.552013769e-01f,-6.554928530e-01f,
1.000000000e+00f,0.000000000e+00f,7.531867990e-01f,-6.578066933e-01f,1.000000000e+00f,0.000000000e+00f,7.511651319e-01f,-6.601143421e-01f,
1.000000000e+00f,0.000000000e+00f,7.491363945e-01f,-6.624157776e-01f,1.000000000e+00f,0.000000000e+00f,7.471006060e-01f,-6.647109782e-01f,
1.000000000e+00f,0.000000000e+00f,7.450577854e-01f,-6.669999223e-01f,1.000000000e+00f,0.000000000e+00f,7.430079521e-01f,-6.692825883e-01f,
1.000000000e+00f,0.000000000e+00f,7.409511254e-01f,-6.715589548e-01f,1.000000000e+00f,0.000000000e+00f,7.388873245e-01f,-6.738290004e-01f,
1.000000000e+00f,0.000000000e+00f,7.368165689e-01f,-6.760927036e-01f,1.000000000e+00f,0.000000000e+00f,7.347388781e-01f,-6.783500431e-01f,
1.000000000e+00f,0.000000000e+00f,7.326542717e-01f,-6.806009978e-01f,1.000000000e+00f,0.000000000e+00f,7.305627692e-01f,-6.828455464e-01f,
1.000000000e+00f,0.000000000e+00f,7.284643904e-01f,-6.850836678e-01f,1.000000000e+00f,0.000000000e+00f,7.263591551e-01f,-6.873153409e-01f,
1.000000000e+00f,0.000000000e+00f,7.242470830e-01f,-6.895405447e-01f,1.000000000e+00f,0.000000000e+00f,7.221281939e-01f,-6.917592584e-01f,
1.000000000e+00f,0.000000000e+00f,7.200025080e-01f,-6.939714609e-01f,1.000000000e+00f,0.000000000e+00f,7.178700451e-01f,-6.961771315e-01f,
1.000000000e+00f,0.000000000e+00f,7.157308253e-01f,-6.983762494e-01f,1.000000000e+00f,0.000000000e+00f,7.135848688e-01f,-7.005687939e-01f,
1.000000000e+00f,0.000000000e+00f,7.114321957e-01f,-7.027547445e-01f,1.000000000e+00f,0.000000000e+00f,7.092728264e-01f,-7.049340804e-01f,
1.000000000e+00f,0.000000000e+00f,1.000000000e+00f,0.000000000e+00f,9.999811753e-01f,-6.135884649e-03f,9.999576446e-01f,-9.203754782e-03f,
9.999247018e-01f,-1.227153829e-02f,9.998305818e-01f,-1.840672991e-02f,9.998305818e-01f,-1.840672991e-02f,9.996188225e-01f,-2.760814578e-02f,
9.996988187e-01f,-2.454122852e-02f,9.993223846e-01f,-3.680722294e-02f,9.995294175e-01f,-3.067480318e-02f,9.989412932e-01f,-4.600318213e-02f,
9.993223846e-01f,-3.680722294e-02f,9.984755806e-01f,-5.519524435e-02f,9.990777278e-01f,-4.293825693e-02f,9.979252862e-01f,-6.438263093e-02f,
9.987954562e-01f,-4.906767433e-02f,9.972904567e-01f,-7.356456360e-02f,9.984755806e-01f,-5.519524435e-02f,9.965711458e-01f,-8.274026455e-02f,
9.981181129e-01f,-6.132073630e-02f,9.957674145e-01f,-9.190895650e-02f,9.977230666e-01f,-6.744391956e-02f,9.948793308e-01f,-1.010698628e-01f,
9.972904567e-01f,-7.356456360e-02f,9.939069700e-01f,-1.102222073e-01f,9.968202993e-01f,-7.968243797e-02f,9.928504145e-01f,-1.193652148e-01f,
9.963126122e-01f,-8.579731234e-02f,9.917097537e-01f,-1.284981108e-01f,9.957674145e-01f,-9.190895650e-02f,9.904850843e-01f,-1.376201216e-01f,
9.951847267e-01f,-9.801714033e-02f,9.891765100e-01f,-1.467304745e-01f,9.945645707e-01f,-1.041216339e-01f,9.877841416e-01f,-1.558283977e-01f,
9.939069700e-01f,-1.102222073e-01f,9.863080972e-01f,-1.649131205e-01f,9.932119492e-01f,-1.163186309e-01f,9.847485018e-01f,-1.739838734e-01f,
9.924795346e-01f,-1.224106752e-01f,9.831054874e-01f,-1.830398880e-01f,9.917097537e-01f,-1.284981108e-01f,9.813791933e-01f,-1.920803970e-01f,
9.909026354e-01f,-1.345807085e-01f,9.795697657e-01f,-2.011046348e-01f,9.900582103e-01f,-1.406582393e-01f,9.776773578e-01f,-2.101118369e-01f,
9.891765100e-01f,-1.467304745e-01f,9.757021300e-01f,-2.191012402e-01f,9.882575677e-01f,-1.527971853e-01f,9.736442497e-01f,-2.280720832e-01f,
9.873014182e-01f,-1.588581433e-01f,9.715038910e-01f,-2.370236060e-01f,9.863080972e-01f,-1.649131205e-01f,9.692812354e-01f,-2.459550503e-01f,
9.852776424e-01f,-1.709618888e-01f,9.669764710e-01f,-2.548656596e-01f,9.842100924e-01f,-1.770042204e-01f,9.645897933e-01f,-2.637546790e-01f,
9.831054874e-01f,-1.830398880e-01f,9.621214043e-01f,-2.726213554e-01f,9.819638691e-01f,-1.890686641e-01f,9.595715131e-01f,-2.814649379e-01f,
9.807852804e-01f,-1.950903220e-01f,9.569403357e-01f,-2.902846773e-01f,9.795697657e-01f,-2.011046348e-01f,9.542280951e-01f,-2.990798263e-01f,
9.783173707e-01f,-2.071113762e-01f,9.514350210e-01f,-3.078496400e-01f,9.770281427e-01f,-2.131103199e-01f,9.485613499e-01f,-3.165933756e-01f,
9.757021300e-01f,-2.191012402e-01f,9.456073254e-01f,-3.253102922e-01f,9.743393828e-01f,-2.250839114e-01f,9.425731976e-01f,-3.339996514e-01f,
9.729399522e-01f,-2.310581083e-01f,9.394592236e-01f,-3.426607173e-01f,9.715038910e-01f,-2.370236060e-01f,9.362656672e-01f,-3.512927561e-01f,
9.700312532e-01f,-2.429801799e-01f,9.329927988e-01f,-3.598950365e-01f,9.685220943e-01f,-2.489276057e-01f,9.296408958e-01f,-3.684668300e-01f,
9.669764710e-01f,-2.548656596e-01f,9.262102421e-01f,-3.770074102e-01f,9.653944417e-01f,-2.607941179e-01f,9.227011283e-01f,-3.855160538e-01f,
9.637760658e-01f,-2.667127575e-01f,9.191138517e-01f,-3.939920401e-01f,9.621214043e-01f,-2.726213554e-01f,9.154487161e-01f,-4.024346509e-01f,
9.604305194e-01f,-2.785196894e-01f,9.117060320e-01f,-4.108431711e-01f,9.587034749e-01f,-2.844075372e-01f,9.078861165e-01f,-4.192168884e-01f,
9.569403357e-01f,-2.902846773e-01f,9.039892931e-01f,-4.275550934e-01f,9.551411683e-01f,-2.961508882e-01f,9.000158920e-01f,-4.358570799e-01f,
9.533060404e-01f,-3.020059493e-01f,8.959662498e-01f,-4.441221446e-01f,9.514350210e-01f,-3.078496400e-01f,8.918407094e-01f,-4.523495872e-01f,
9.495281806e-01f,-3.136817404e-01f,8.876396204e-01f,-4.605387110e-01f,9.475855910e-01f,-3.195020308e-01f,8.833633387e-01f,-4.686888220e-01f,
9.456073254e-01f,-3.253102922e-01f,8.790122264e-01f,-4.767992301e-01f,9.435934582e-01f,-3.311063058e-01f,8.745866523e-01f,-4.848692480e-01f,
9.415440652e-01f,-3.368898534e-01f,8.700869911e-01f,-4.928981922e-01f,9.394592236e-01f,-3.426607173e-01f,8.655136241e-01f,-5.008853826e-01f,
9.373390119e-01f,-3.484186802e-01f,8.608669386e-01f,-5.088301425e-01f,9.351835099e-01f,-3.541635254e-01f,8.561473284e-01f,-5.167317990e-01f,
9.329927988e-01f,-3.598950365e-01f,8.513551931e-01f,-5.245896827e-01f,9.307669611e-01f,-3.656129978e-01f,8.464909388e-01f,-5.324031279e-01f,
9.285060805e-01f,-3.713171940e-01f,8.415549774e-01f,-5.401714727e-01f,9.262102421e-01f,-3.770074102e-01f,8.365477272e-01f,-5.478940592e-01f,
9.238795325e-01f,-3.826834324e-01f,8.314696123e-01f,-5.555702330e-01f,9.215140393e-01f,-3.883450467e-01f,8.263210628e-01f,-5.631993440e-01f,
9.191138517e-01f,-3.939920401e-01f,8.211025150e-01f,-5.707807459e-01f,9.166790599e-01f,-3.996241998e-01f,8.158144108e-01f,-5.783137964e-01f,
9.142097557e-01f,-4.052413140e-01f,8.104571983e-01f,-5.857978575e-01f,9.117060320e-01f,-4.108431711e-01f,8.050313311e-01f,-5.932322950e-01f,
9.091679831e-01f,-4.164295601e-01f,7.995372691e-01f,-6.006164794e-01f,9.065957045e-01f,-4.220002708e-01f,7.939754776e-01f,-6.079497850e-01f,
9.039892931e-01f,-4.275550934e-01f,7.883464276e-01f,-6.152315906e-01f,9.013488470e-01f,-4.330938189e-01f,7.826505962e-01f,-6.224612794e-01f,
8.986744657e-01f,-4.386162385e-01f,7.768884657e-01f,-6.296382389e-01f,8.959662498e-01f,-4.441221446e-01f,7.710605243e-01f,-6.367618612e-01f,
8.932243012e-01f,-4.496113297e-01f,7.651672656e-01f,-6.438315429e-01f,8.904487232e-01f,-4.550835871e-01f,7.592091890e-01f,-6.508466850e-01f,
8.876396204e-01f,-4.605387110e-01f,7.531867990e-01f,-6.578066933e-01f,8.847970984e-01f,-4.659764958e-01f,7.471006060e-01f,-6.647109782e-01f,
8.819212643e-01f,-4.713967368e-01f,7.409511254e-01f,-6.715589548e-01f,8.790122264e-01f,-4.767992301e-01f,7.347388781e-01f,-6.783500431e-01f,
8.760700942e-01f,-4.821837721e-01f,7.284643904e-01f,-6.850836678e-01f,8.730949784e-01f,-4.875501601e-01f,7.221281939e-01f,-6.917592584e-01f,
8.700869911e-01f,-4.928981922e-01f,7.157308253e-01f,-6.983762494e-01f,8.670462455e-01f,-4.982276670e-01f,7.092728264e-01f,-7.049340804e-01f,
8.639728561e-01f,-5.035383837e-01f,7.027547445e-01f,-7.114321957e-01f,8.608669386e-01f,-5.088301425e-01f,6.961771315e-01f,-7.178700451e-01f,
8.577286100e-01f,-5.141027442e-01f,6.895405447e-01f,-7.242470830e-01f,8.545579884e-01f,-5.193559902e-01f,6.828455464e-01f,-7.305627692e-01f,
8.513551931e-01f,-5.245896827e-01f,6.760927036e-01f,-7.368165689e-01f,8.481203448e-01f,-5.298036247e-01f,6.692825883e-01f,-7.430079521e-01f,
8.448535652e-01f,-5.349976199e-01f,6.624157776e-01f,-7.491363945e-01f,8.415549774e-01f,-5.401714727e-01f,6.554928530e-01f,-7.552013769e-01f,
8.382247056e-01f,-5.453249884e-01f,6.485144010e-01f,-7.612023855e-01f,8.348628750e-01f,-5.504579729e-01f,6.414810128e-01f,-7.671389119e-01f,
8.314696123e-01f,-5.555702330e-01f,6.343932842e-01f,-7.730104534e-01f,8.280450453e-01f,-5.606615762e-01f,6.272518155e-01f,-7.788165124e-01f,
8.245893028e-01f,-5.657318108e-01f,6.200572118e-01f,-7.845565972e-01f,8.211025150e-01f,-5.707807459e-01f,6.128100824e-01f,-7.902302214e-01f,
8.175848132e-01f,-5.758081914e-01f,6.055110414e-01f,-7.958369046e-01f,8.140363297e-01f,-5.808139581e-01f,5.981607070e-01f,-8.013761717e-01f,
8.104571983e-01f,-5.857978575e-01f,5.907597019e-01f,-8.068475535e-01f,8.068475535e-01f,-5.907597019e-01f,5.833086529e-01f,-8.122505866e-01f,
8.032075315e-01f,-5.956993045e-01f,5.758081914e-01f,-8.175848132e-01f,7.995372691e-01f,-6.006164794e-01f,5.682589527e-01f,-8.228497814e-01f,
7.958369046e-01f,-6.055110414e-01f,5.606615762e-01f,-8.280450453e-01f,7.921065773e-01f,-6.103828063e-01f,5.530167056e-01f,-8.331701647e-01f,
7.883464276e-01f,-6.152315906e-01f,5.453249884e-01f,-8.382247056e-01f,7.845565972e-01f,-6.200572118e-01f,5.375870763e-01f,-8.432082396e-01f,
7.807372286e-01f,-6.248594881e-01f,5.298036247e-01f,-8.481203448e-01f,7.768884657e-01f,-6.296382389e-01f,5.219752929e-01f,-8.529606049e-01f,
7.730104534e-01f,-6.343932842e-01f,5.141027442e-01f,-8.577286100e-01f,7.691033376e-01f,-6.391244449e-01f,5.061866453e-01f,-8.624239561e-01f,
7.651672656e-01f,-6.438315429e-01f,4.982276670e-01f,-8.670462455e-01f,7.612023855e-01f,-6.485144010e-01f,4.902264833e-01f,-8.715950867e-01f,
7.572088465e-01f,-6.531728430e-01f,4.821837721e-01f,-8.760700942e-01f,7.531867990e-01f,-6.578066933e-01f,4.741002147e-01f,-8.804708891e-01f,
7.491363945e-01f,-6.624157776e-01f,4.659764958e-01f,-8.847970984e-01f,7.450577854e-01f,-6.669999223e-01f,4.578133036e-01f,-8.890483559e-01f,
7.409511254e-01f,-6.715589548e-01f,4.496113297e-01f,-8.932243012e-01f,7.368165689e-01f,-6.760927036e-01f,4.413712687e-01f,-8.973245807e-01f,
7.326542717e-01f,-6.806009978e-01f,4.330938189e-01f,-9.013488470e-01f,7.284643904e-01f,-6.850836678e-01f,4.247796812e-01f,-9.052967593e-01f,
7.242470830e-01f,-6.895405447e-01f,4.164295601e-01f,-9.091679831e-01f,7.200025080e-01f,-6.939714609e-01f,4.080441629e-01f,-9.129621904e-01f,
7.157308253e-01f,-6.983762494e-01f,3.996241998e-01f,-9.166790599e-01f,7.114321957e-01f,-7.027547445e-01f,3.911703843e-01f,-9.203182767e-01f,
7.071067812e-01f,-7.071067812e-01f,3.826834324e-01f,-9.238795325e-01f,7.027547445e-01f,-7.114321957e-01f,3.741640630e-01f,-9.273625257e-01f,
6.983762494e-01f,-7.157308253e-01f,3.656129978e-01f,-9.307669611e-01f,6.939714609e-01f,-7.200025080e-01f,3.570309612e-01f,-9.340925504e-01f,
6.895405447e-01f,-7.242470830e-01f,3.484186802e-01f,-9.373390119e-01f,6.850836678e-01f,-7.284643904e-01f,3.397768844e-01f,-9.405060706e-01f,
6.806009978e-01f,-7.326542717e-01f,3.311063058e-01f,-9.435934582e-01f,6.760927036e-01f,-7.368165689e-01f,3.224076788e-01f,-9.466009131e-01f,
6.715589548e-01f,-7.409511254e-01f,3.136817404e-01f,-9.495281806e-01f,6.669999223e-01f,-7.450577854e-01f,3.049292297e-01f,-9.523750127e-01f,
6.624157776e-01f,-7.491363945e-01f,2.961508882e-01f,-9.551411683e-01f,6.578066933e-01f,-7.531867990e-01f,2.873474595e-01f,-9.578264130e-01f,
6.531728430e-01f,-7.572088465e-01f,2.785196894e-01f,-9.604305194e-01f,6.485144010e-01f,-7.612023855e-01f,2.696683256e-01f,-9.629532669e-01f,
6.438315429e-01f,-7.651672656e-01f,2.607941179e-01f,-9.653944417e-01f,6.391244449e-01f,-7.691033376e-01f,2.518978182e-01f,-9.677538371e-01f,
6.343932842e-01f,-7.730104534e-01f,2.429801799e-01f,-9.700312532e-01f,6.296382389e-01f,-7.768884657e-01f,2.340419586e-01f,-9.722264971e-01f,
6.248594881e-01f,-7.807372286e-01f,2.250839114e-01f,-9.743393828e-01f,6.200572118e-01f,-7.845565972e-01f,2.161067971e-01f,-9.763697313e-01f,
6.152315906e-01f,-7.883464276e-01f,2.071113762e-01f,-9.783173707e-01f,6.103828063e-01f,-7.921065773e-01f,1.980984107e-01f,-9.801821360e-01f,
6.055110414e-01f,-7.958369046e-01f,1.890686641e-01f,-9.819638691e-01f,6.006164794e-01f,-7.995372691e-01f,1.800229014e-01f,-9.836624192e-01f,
5.956993045e-01f,-8.032075315e-01f,1.709618888e-01f,-9.852776424e-01f,5.907597019e-01f,-8.068475535e-01f,1.618863938e-01f,-9.868094018e-01f,
5.857978575e-01f,-8.104571983e-01f,1.527971853e-01f,-9.882575677e-01f,5.808139581e-01f,-8.140363297e-01f,1.436950332e-01f,-9.896220175e-01f,
5.758081914e-01f,-8.175848132e-01f,1.345807085e-01f,-9.909026354e-01f,5.707807459e-01f,-8.211025150e-01f,1.254549834e-01f,-9.920993131e-01f,
5.657318108e-01f,-8.245893028e-01f,1.163186309e-01f,-9.932119492e-01f,5.606615762e-01f,-8.280450453e-01f,1.071724250e-01f,-9.942404495e-01f,
5.555702330e-01f,-8.314696123e-01f,9.801714033e-02f,-9.951847267e-01f,5.504579729e-01f,-8.348628750e-01f,8.885355258e-02f,-9.960447009e-01f,
5.453249884e-01f,-8.382247056e-01f,7.968243797e-02f,-9.968202993e-01f,5.401714727e-01f,-8.415549774e-01f,7.050457339e-02f,-9.975114561e-01f,
5.349976199e-01f,-8.448535652e-01f,6.132073630e-02f,-9.981181129e-01f,5.298036247e-01f,-8.481203448e-01f,5.213170468e-02f,-9.986402182e-01f,
5.245896827e-01f,-8.513551931e-01f,4.293825693e-02f,-9.990777278e-01f,5.193559902e-01f,-8.545579884e-01f,3.374117185e-02f,-9.994306046e-01f,
5.141027442e-01f,-8.577286100e-01f,2.454122852e-02f,-9.996988187e-01f,5.088301425e-01f,-8.608669386e-01f,1.533920628e-02f,-9.998823475e-01f,
5.035383837e-01f,-8.639728561e-01f,6.135884649e-03f,-9.999811753e-01f,4.982276670e-01f,-8.670462455e-01f,-3.067956763e-03f,-9.999952938e-01f,
4.928981922e-01f,-8.700869911e-01f,-1.227153829e-02f,-9.999247018e-01f,4.875501601e-01f,-8.730949784e-01f,-2.147408028e-02f,-9.997694054e-01f,
4.821837721e-01f,-8.760700942e-01f,-3.067480318e-02f,-9.995294175e-01f,4.767992301e-01f,-8.790122264e-01f,-3.987292759e-02f,-9.992047586e-01f,
4.713967368e-01f,-8.819212643e-01f,-4.906767433e-02f,-9.987954562e-01f,4.659764958e-01f,-8.847970984e-01f,-5.825826450e-02f,-9.983015449e-01f,
4.605387110e-01f,-8.876396204e-01f,-6.744391956e-02f,-9.977230666e-01f,4.550835871e-01f,-8.904487232e-01f,-7.662386139e-02f,-9.970600703e-01f,
4.496113297e-01f,-8.932243012e-01f,-8.579731234e-02f,-9.963126122e-01f,4.441221446e-01f,-8.959662498e-01f,-9.496349533e-02f,-9.954807555e-01f,
4.386162385e-01f,-8.986744657e-01f,-1.041216339e-01f,-9.945645707e-01f,4.330938189e-01f,-9.013488470e-01f,-1.132709522e-01f,-9.935641355e-01f,
4.275550934e-01f,-9.039892931e-01f,-1.224106752e-01f,-9.924795346e-01f,4.220002708e-01f,-9.065957045e-01f,-1.315400287e-01f,-9.913108598e-01f,
4.164295601e-01f,-9.091679831e-01f,-1.406582393e-01f,-9.900582103e-01f,4.108431711e-01f,-9.117060320e-01f,-1.497645347e-01f,-9.887216920e-01f,
4.052413140e-01f,-9.142097557e-01f,-1.588581433e-01f,-9.873014182e-01f,3.996241998e-01f,-9.166790599e-01f,-1.679382950e-01f,-9.857975092e-01f,
3.939920401e-01f,-9.191138517e-01f,-1.770042204e-01f,-9.842100924e-01f,3.883450467e-01f,-9.215140393e-01f,-1.860551517e-01f,-9.825393023e-01f,
3.826834324e-01f,-9.238795325e-01f,-1.950903220e-01f,-9.807852804e-01f,3.770074102e-01f,-9.262102421e-01f,-2.041089661e-01f,-9.789481753e-01f,
3.713171940e-01f,-9.285060805e-01f,-2.131103199e-01f,-9.770281427e-01f,3.656129978e-01f,-9.307669611e-01f,-2.220936210e-01f,-9.750253451e-01f,
3.598950365e-01f,-9.329927988e-01f,-2.310581083e-01f,-9.729399522e-01f,3.541635254e-01f,-9.351835099e-01f,-2.400030224e-01f,-9.707721407e-01f,
3.484186802e-01f,-9.373390119e-01f,-2.489276057e-01f,-9.685220943e-01f,3.426607173e-01f,-9.394592236e-01f,-2.578311022e-01f,-9.661900034e-01f,
3.368898534e-01f,-9.415440652e-01f,-2.667127575e-01f,-9.637760658e-01f,3.311063058e-01f,-9.435934582e-01f,-2.755718193e-01f,-9.612804858e-01f,
3.253102922e-01f,-9.456073254e-01f,-2.844075372e-01f,-9.587034749e-01f,3.195020308e-01f,-9.475855910e-01f,-2.932191627e-01f,-9.560452513e-01f,
3.136817404e-01f,-9.495281806e-01f,-3.020059493e-01f,-9.533060404e-01f,3.078496400e-01f,-9.514350210e-01f,-3.107671527e-01f,-9.504860739e-01f,
3.020059493e-01f,-9.533060404e-01f,-3.195020308e-01f,-9.475855910e-01f,2.961508882e-01f,-9.551411683e-01f,-3.282098436e-01f,-9.446048373e-01f,
2.902846773e-01f,-9.569403357e-01f,-3.368898534e-01f,-9.415440652e-01f,2.844075372e-01f,-9.587034749e-01f,-3.455413250e-01f,-9.384035341e-01f,
2.785196894e-01f,-9.604305194e-01f,-3.541635254e-01f,-9.351835099e-01f,2.726213554e-01f,-9.621214043e-01f,-3.627557244e-01f,-9.318842656e-01f,
2.667127575e-01f,-9.637760658e-01f,-3.713171940e-01f,-9.285060805e-01f,2.607941179e-01f,-9.653944417e-01f,-3.798472089e-01f,-9.250492408e-01f,
2.548656596e-01f,-9.669764710e-01f,-3.883450467e-01f,-9.215140393e-01f,2.489276057e-01f,-9.685220943e-01f,-3.968099874e-01f,-9.179007756e-01f,
2.429801799e-01f,-9.700312532e-01f,-4.052413140e-01f,-9.142097557e-01f,2.370236060e-01f,-9.715038910e-01f,-4.136383122e-01f,-9.104412923e-01f,
2.310581083e-01f,-9.729399522e-01f,-4.220002708e-01f,-9.065957045e-01f,2.250839114e-01f,-9.743393828e-01f,-4.303264813e-01f,-9.026733182e-01f,
2.191012402e-01f,-9.757021300e-01f,-4.386162385e-01f,-8.986744657e-01f,2.131103199e-01f,-9.770281427e-01f,-4.468688402e-01f,-8.945994856e-01f,
2.071113762e-01f,-9.783173707e-01f,-4.550835871e-01f,-8.904487232e-01f,2.011046348e-01f,-9.795697657e-01f,-4.632597836e-01f,-8.862225301e-01f,
1.950903220e-01f,-9.807852804e-01f,-4.713967368e-01f,-8.819212643e-01f,1.890686641e-01f,-9.819638691e-01f,-4.794937577e-01f,-8.775452902e-01f,
1.830398880e-01f,-9.831054874e-01f,-4.875501601e-01f,-8.730949784e-01f,1.770042204e-01f,-9.842100924e-01f,-4.955652618e-01f,-8.685707060e-01f,
1.709618888e-01f,-9.852776424e-01f,-5.035383837e-01f,-8.639728561e-01f,1.649131205e-01f,-9.863080972e-01f,-5.114688504e-01f,-8.593018184e-01f,
1.588581433e-01f,-9.873014182e-01f,-5.193559902e-01f,-8.545579884e-01f,1.527971853e-01f,-9.882575677e-01f,-5.271991348e-01f,-8.497417680e-01f,
1.467304745e-01f,-9.891765100e-01f,-5.349976199e-01f,-8.448535652e-01f,1.406582393e-01f,-9.900582103e-01f,-5.427507849e-01f,-8.398937942e-01f,
1.345807085e-01f,-9.909026354e-01f,-5.504579729e-01f,-8.348628750e-01f,1.284981108e-01f,-9.917097537e-01f,-5.581185312e-01f,-8.297612338e-01f,
1.224106752e-01f,-9.924795346e-01f,-5.657318108e-01f,-8.245893028e-01f,1.163186309e-01f,-9.932119492e-01f,-5.732971667e-01f,-8.193475201e-01f,
1.102222073e-01f,-9.939069700e-01f,-5.808139581e-01f,-8.140363297e-01f,1.041216339e-01f,-9.945645707e-01f,-5.882815482e-01f,-8.086561816e-01f,
9.801714033e-02f,-9.951847267e-01f,-5.956993045e-01f,-8.032075315e-01f,9.190895650e-02f,-9.957674145e-01f,-6.030665985e-01f,-7.976908409e-01f,
8.579731234e-02f,-9.963126122e-01f,-6.103828063e-01f,-7.921065773e-01f,7.968243797e-02f,-9.968202993e-01f,-6.176473079e-01f,-7.864552136e-01f,
7.356456360e-02f,-9.972904567e-01f,-6.248594881e-01f,-7.807372286e-01f,6.744391956e-02f,-9.977230666e-01f,-6.320187359e-01f,-7.749531066e-01f,
6.132073630e-02f,-9.981181129e-01f,-6.391244449e-01f,-7.691033376e-01f,5.519524435e-02f,-9.984755806e-01f,-6.461760130e-01f,-7.631884173e-01f,
4.906767433e-02f,-9.987954562e-01f,-6.531728430e-01f,-7.572088465e-01f,4.293825693e-02f,-9.990777278e-01f,-6.601143421e-01f,-7.511651319e-01f,
3.680722294e-02f,-9.993223846e-01f,-6.669999223e-01f,-7.450577854e-01f,3.067480318e-02f,-9.995294175e-01f,-6.738290004e-01f,-7.388873245e-01f,
2.454122852e-02f,-9.996988187e-01f,-6.806009978e-01f,-7.326542717e-01f,1.840672991e-02f,-9.998305818e-01f,-6.873153409e-01f,-7.263591551e-01f,
1.227153829e-02f,-9.999247018e-01f,-6.939714609e-01f,-7.200025080e-01f,6.135884649e-03f,-9.999811753e-01f,-7.005687939e-01f,-7.135848688e-01f,
1.000000000e+00f,0.000000000e+00f,1.000000000e+00f,0.000000000e+00f,9.999247018e-01f,-1.227153829e-02f,9.998823475e-01f,-1.533920628e-02f,
9.996988187e-01f,-2.454122852e-02f,9.995294175e-01f,-3.067480318e-02f,9.993223846e-01f,-3.680722294e-02f,9.989412932e-01f,-4.600318213e-02f,
9.987954562e-01f,-4.906767433e-02f,9.981181129e-01f,-6.132073630e-02f,9.981181129e-01f,-6.132073630e-02f,9.970600703e-01f,-7.662386139e-02f,
9.972904567e-01f,-7.356456360e-02f,9.957674145e-01f,-9.190895650e-02f,9.963126122e-01f,-8.579731234e-02f,9.942404495e-01f,-1.071724250e-01f,
9.951847267e-01f,-9.801714033e-02f,9.924795346e-01f,-1.224106752e-01f,9.939069700e-01f,-1.102222073e-01f,9.904850843e-01f,-1.376201216e-01f,
9.924795346e-01f,-1.224106752e-01f,9.882575677e-01f,-1.527971853e-01f,9.909026354e-01f,-1.345807085e-01f,9.857975092e-01f,-1.679382950e-01f,
9.891765100e-01f,-1.467304745e-01f,9.831054874e-01f,-1.830398880e-01f,9.873014182e-01f,-1.588581433e-01f,9.801821360e-01f,-1.980984107e-01f,
9.852776424e-01f,-1.709618888e-01f,9.770281427e-01f,-2.131103199e-01f,9.831054874e-01f,-1.830398880e-01f,9.736442497e-01f,-2.280720832e-01f,
9.807852804e-01f,-1.950903220e-01f,9.700312532e-01f,-2.429801799e-01f,9.783173707e-01f,-2.071113762e-01f,9.661900034e-01f,-2.578311022e-01f,
9.757021300e-01f,-2.191012402e-01f,9.621214043e-01f,-2.726213554e-01f,9.729399522e-01f,-2.310581083e-01f,9.578264130e-01f,-2.873474595e-01f,
9.700312532e-01f,-2.429801799e-01f,9.533060404e-01f,-3.020059493e-01f,9.669764710e-01f,-2.548656596e-01f,9.485613499e-01f,-3.165933756e-01f,
9.637760658e-01f,-2.667127575e-01f,9.435934582e-01f,-3.311063058e-01f,9.604305194e-01f,-2.785196894e-01f,9.384035341e-01f,-3.455413250e-01f,
9.569403357e-01f,-2.902846773e-01f,9.329927988e-01f,-3.598950365e-01f,9.533060404e-01f,-3.020059493e-01f,9.273625257e-01f,-3.741640630e-01f,
9.495281806e-01f,-3.136817404e-01f,9.215140393e-01f,-3.883450467e-01f,9.456073254e-01f,-3.253102922e-01f,9.154487161e-01f,-4.024346509e-01f,
9.415440652e-01f,-3.368898534e-01f,9.091679831e-01f,-4.164295601e-01f,9.373390119e-01f,-3.484186802e-01f,9.026733182e-01f,-4.303264813e-01f,
9.329927988e-01f,-3.598950365e-01f,8.959662498e-01f,-4.441221446e-01f,9.285060805e-01f,-3.713171940e-01f,8.890483559e-01f,-4.578133036e-01f,
9.238795325e-01f,-3.826834324e-01f,8.819212643e-01f,-4.713967368e-01f,9.191138517e-01f,-3.939920401e-01f,8.745866523e-01f,-4.848692480e-01f,
9.142097557e-01f,-4.052413140e-01f,8.670462455e-01f,-4.982276670e-01f,9.091679831e-01f,-4.164295601e-01f,8.593018184e-01f,-5.114688504e-01f,
9.039892931e-01f,-4.275550934e-01f,8.513551931e-01f,-5.245896827e-01f,8.986744657e-01f,-4.386162385e-01f,8.432082396e-01f,-5.375870763e-01f,
8.932243012e-01f,-4.496113297e-01f,8.348628750e-01f,-5.504579729e-01f,8.876396204e-01f,-4.605387110e-01f,8.263210628e-01f,-5.631993440e-01f,
8.819212643e-01f,-4.713967368e-01f,8.175848132e-01f,-5.758081914e-01f,8.760700942e-01f,-4.821837721e-01f,8.086561816e-01f,-5.882815482e-01f,
8.700869911e-01f,-4.928981922e-01f,7.995372691e-01f,-6.006164794e-01f,8.639728561e-01f,-5.035383837e-01f,7.902302214e-01f,-6.128100824e-01f,
8.577286100e-01f,-5.141027442e-01f,7.807372286e-01f,-6.248594881e-01f,8.513551931e-01f,-5.245896827e-01f,7.710605243e-01f,-6.367618612e-01f,
8.448535652e-01f,-5.349976199e-01f,7.612023855e-01f,-6.485144010e-01f,8.382247056e-01f,-5.453249884e-01f,7.511651319e-01f,-6.601143421e-01f,
8.314696123e-01f,-5.555702330e-01f,7.409511254e-01f,-6.715589548e-01f,8.245893028e-01f,-5.657318108e-01f,7.305627692e-01f,-6.828455464e-01f,
8.175848132e-01f,-5.758081914e-01f,7.200025080e-01f,-6.939714609e-01f,8.104571983e-01f,-5.857978575e-01f,7.092728264e-01f,-7.049340804e-01f,
8.032075315e-01f,-5.956993045e-01f,6.983762494e-01f,-7.157308253e-01f,7.958369046e-01f,-6.055110414e-01f,6.873153409e-01f,-7.263591551e-01f,
7.883464276e-01f,-6.152315906e-01f,6.760927036e-01f,-7.368165689e-01f,7.807372286e-01f,-6.248594881e-01f,6.647109782e-01f,-7.471006060e-01f,
7.730104534e-01f,-6.343932842e-01f,6.531728430e-01f,-7.572088465e-01f,7.651672656e-01f,-6.438315429e-01f,6.414810128e-01f,-7.671389119e-01f,
7.572088465e-01f,-6.531728430e-01f,6.296382389e-01f,-7.768884657e-01f,7.491363945e-01f,-6.624157776e-01f,6.176473079e-01f,-7.864552136e-01f,
7.409511254e-01f,-6.715589548e-01f,6.055110414e-01f,-7.958369046e-01f,7.326542717e-01f,-6.806009978e-01f,5.932322950e-01f,-8.050313311e-01f,
7.242470830e-01f,-6.895405447e-01f,5.808139581e-01f,-8.140363297e-01f,7.157308253e-01f,-6.983762494e-01f,5.682589527e-01f,-8.228497814e-01f,
7.071067812e-01f,-7.071067812e-01f,5.555702330e-01f,-8.314696123e-01f,6.983762494e-01f,-7.157308253e-01f,5.427507849e-01f,-8.398937942e-01f,
6.895405447e-01f,-7.242470830e-01f,5.298036247e-01f,-8.481203448e-01f,6.806009978e-01f,-7.326542717e-01f,5.167317990e-01f,-8.561473284e-01f,
6.715589548e-01f,-7.409511254e-01f,5.035383837e-01f,-8.639728561e-01f,6.624157776e-01f,-7.491363945e-01f,4.902264833e-01f,-8.715950867e-01f,
6.531728430e-01f,-7.572088465e-01f,4.767992301e-01f,-8.790122264e-01f,6.438315429e-01f,-7.651672656e-01f,4.632597836e-01f,-8.862225301e-01f,
6.343932842e-01f,-7.730104534e-01f,4.496113297e-01f,-8.932243012e-01f,6.248594881e-01f,-7.807372286e-01f,4.358570799e-01f,-9.000158920e-01f,
6.152315906e-01f,-7.883464276e-01f,4.220002708e-01f,-9.065957045e-01f,6.055110414e-01f,-7.958369046e-01f,4.080441629e-01f,-9.129621904e-01f,
5.956993045e-01f,-8.032075315e-01f,3.939920401e-01f,-9.191138517e-01f,5.857978575e-01f,-8.104571983e-01f,3.798472089e-01f,-9.250492408e-01f,
5.758081914e-01f,-8.175848132e-01f,3.656129978e-01f,-9.307669611e-01f,5.657318108e-01f,-8.245893028e-01f,3.512927561e-01f,-9.362656672e-01f,
5.555702330e-01f,-8.314696123e-01f,3.368898534e-01f,-9.415440652e-01f,5.453249884e-01f,-8.382247056e-01f,3.224076788e-01f,-9.466009131e-01f,
5.349976199e-01f,-8.448535652e-01f,3.078496400e-01f,-9.514350210e-01f,5.245896827e-01f,-8.513551931e-01f,2.932191627e-01f,-9.560452513e-01f,
5.141027442e-01f,-8.577286100e-01f,2.785196894e-01f,-9.604305194e-01f,5.035383837e-01f,-8.639728561e-01f,2.637546790e-01f,-9.645897933e-01f,
4.928981922e-01f,-8.700869911e-01f,2.489276057e-01f,-9.685220943e-01f,4.821837721e-01f,-8.760700942e-01f,2.340419586e-01f,-9.722264971e-01f,
4.713967368e-01f,-8.819212643e-01f,2.191012402e-01f,-9.757021300e-01f,4.605387110e-01f,-8.876396204e-01f,2.041089661e-01f,-9.789481753e-01f,
4.496113297e-01f,-8.932243012e-01f,1.890686641e-01f,-9.819638691e-01f,4.386162385e-01f,-8.986744657e-01f,1.739838734e-01f,-9.847485018e-01f,
4.275550934e-01f,-9.039892931e-01f,1.588581433e-01f,-9.873014182e-01f,4.164295601e-01f,-9.091679831e-01f,1.436950332e-01f,-9.896220175e-01f,
4.052413140e-01f,-9.142097557e-01f,1.284981108e-01f,-9.917097537e-01f,3.939920401e-01f,-9.191138517e-01f,1.132709522e-01f,-9.935641355e-01f,
3.826834324e-01f,-9.238795325e-01f,9.801714033e-02f,-9.951847267e-01f,3.713171940e-01f,-9.285060805e-01f,8.274026455e-02f,-9.965711458e-01f,
3.598950365e-01f,-9.329927988e-01f,6.744391956e-02f,-9.977230666e-01f,3.484186802e-01f,-9.373390119e-01f,5.213170468e-02f,-9.986402182e-01f,
3.368898534e-01f,-9.415440652e-01f,3.680722294e-02f,-9.993223846e-01f,3.253102922e-01f,-9.456073254e-01f,2.147408028e-02f,-9.997694054e-01f,
3.136817404e-01f,-9.495281806e-01f,6.135884649e-03f,-9.999811753e-01f,3.020059493e-01f,-9.533060404e-01f,-9.203754782e-03f,-9.999576446e-01f,
2.902846773e-01f,-9.569403357e-01f,-2.454122852e-02f,-9.996988187e-01f,2.785196894e-01f,-9.604305194e-01f,-3.987292759e-02f,-9.992047586e-01f,
2.667127575e-01f,-9.637760658e-01f,-5.519524435e-02f,-9.984755806e-01f,2.548656596e-01f,-9.669764710e-01f,-7.050457339e-02f,-9.975114561e-01f,
2.429801799e-01f,-9.700312532e-01f,-8.579731234e-02f,-9.963126122e-01f,2.310581083e-01f,-9.729399522e-01f,-1.010698628e-01f,-9.948793308e-01f,
2.191012402e-01f,-9.757021300e-01f,-1.163186309e-01f,-9.932119492e-01f,2.071113762e-01f,-9.783173707e-01f,-1.315400287e-01f,-9.913108598e-01f,
1.950903220e-01f,-9.807852804e-01f,-1.467304745e-01f,-9.891765100e-01f,1.830398880e-01f,-9.831054874e-01f,-1.618863938e-01f,-9.868094018e-01f,
1.709618888e-01f,-9.852776424e-01f,-1.770042204e-01f,-9.842100924e-01f,1.588581433e-01f,-9.873014182e-01f,-1.920803970e-01f,-9.813791933e-01f,
1.467304745e-01f,-9.891765100e-01f,-2.071113762e-01f,-9.783173707e-01f,1.345807085e-01f,-9.909026354e-01f,-2.220936210e-01f,-9.750253451e-01f,
1.224106752e-01f,-9.924795346e-01f,-2.370236060e-01f,-9.715038910e-01f,1.102222073e-01f,-9.939069700e-01f,-2.518978182e-01f,-9.677538371e-01f,
9.801714033e-02f,-9.951847267e-01f,-2.667127575e-01f,-9.637760658e-01f,8.579731234e-02f,-9.963126122e-01f,-2.814649379e-01f,-9.595715131e-01f,
7.356456360e-02f,-9.972904567e-01f,-2.961508882e-01f,-9.551411683e-01f,6.132073630e-02f,-9.981181129e-01f,-3.107671527e-01f,-9.504860739e-01f,
4.906767433e-02f,-9.987954562e-01f,-3.253102922e-01f,-9.456073254e-01f,3.680722294e-02f,-9.993223846e-01f,-3.397768844e-01f,-9.405060706e-01f,
2.454122852e-02f,-9.996988187e-01f,-3.541635254e-01f,-9.351835099e-01f,1.227153829e-02f,-9.999247018e-01f,-3.684668300e-01f,-9.296408958e-01f,
0.000000000e+00f,-1.000000000e+00f,-3.826834324e-01f,-9.238795325e-01f,-1.227153829e-02f,-9.999247018e-01f,-3.968099874e-01f,-9.179007756e-01f,
-2.454122852e-02f,-9.996988187e-01f,-4.108431711e-01f,-9.117060320e-01f,-3.680722294e-02f,-9.993223846e-01f,-4.247796812e-01f,-9.052967593e-01f,
-4.906767433e-02f,-9.987954562e-01f,-4.386162385e-01f,-8.986744657e-01f,-6.132073630e-02f,-9.981181129e-01f,-4.523495872e-01f,-8.918407094e-01f,
-7.356456360e-02f,-9.972904567e-01f,-4.659764958e-01f,-8.847970984e-01f,-8.579731234e-02f,-9.963126122e-01f,-4.794937577e-01f,-8.775452902e-01f,
-9.801714033e-02f,-9.951847267e-01f,-4.928981922e-01f,-8.700869911e-01f,-1.102222073e-01f,-9.939069700e-01f,-5.061866453e-01f,-8.624239561e-01f,
-1.224106752e-01f,-9.924795346e-01f,-5.193559902e-01f,-8.545579884e-01f,-1.345807085e-01f,-9.909026354e-01f,-5.324031279e-01f,-8.464909388e-01f,
-1.467304745e-01f,-9.891765100e-01f,-5.453249884e-01f,-8.382247056e-01f,-1.588581433e-01f,-9.873014182e-01f,-5.581185312e-01f,-8.297612338e-01f,
-1.709618888e-01f,-9.852776424e-01f,-5.707807459e-01f,-8.211025150e-01f,-1.830398880e-01f,-9.831054874e-01f,-5.833086529e-01f,-8.122505866e-01f,
-1.950903220e-01f,-9.807852804e-01f,-5.956993045e-01f,-8.032075315e-01f,-2.071113762e-01f,-9.783173707e-01f,-6.079497850e-01f,-7.939754776e-01f,
-2.191012402e-01f,-9.757021300e-01f,-6.200572118e-01f,-7.845565972e-01f,-2.310581083e-01f,-9.729399522e-01f,-6.320187359e-01f,-7.749531066e-01f,
-2.429801799e-01f,-9.700312532e-01f,-6.438315429e-01f,-7.651672656e-01f,-2.548656596e-01f,-9.669764710e-01f,-6.554928530e-01f,-7.552013769e-01f,
-2.667127575e-01f,-9.637760658e-01f,-6.669999223e-01f,-7.450577854e-01f,-2.785196894e-01f,-9.604305194e-01f,-6.783500431e-01f,-7.347388781e-01f,
-2.902846773e-01f,-9.569403357e-01f,-6.895405447e-01f,-7.242470830e-01f,-3.020059493e-01f,-9.533060404e-01f,-7.005687939e-01f,-7.135848688e-01f,
-3.136817404e-01f,-9.495281806e-01f,-7.114321957e-01f,-7.027547445e-01f,-3.253102922e-01f,-9.456073254e-01f,-7.221281939e-01f,-6.917592584e-01f,
-3.368898534e-01f,-9.415440652e-01f,-7.326542717e-01f,-6.806009978e-01f,-3.484186802e-01f,-9.373390119e-01f,-7.430079521e-01f,-6.692825883e-01f,
-3.598950365e-01f,-9.329927988e-01f,-7.531867990e-01f,-6.578066933e-01f,-3.713171940e-01f,-9.285060805e-01f,-7.631884173e-01f,-6.461760130e-01f,
-3.826834324e-01f,-9.238795325e-01f,-7.730104534e-01f,-6.343932842e-01f,-3.939920401e-01f,-9.191138517e-01f,-7.826505962e-01f,-6.224612794e-01f,
-4.052413140e-01f,-9.142097557e-01f,-7.921065773e-01f,-6.103828063e-01f,-4.164295601e-01f,-9.091679831e-01f,-8.013761717e-01f,-5.981607070e-01f,
-4.275550934e-01f,-9.039892931e-01f,-8.104571983e-01f,-5.857978575e-01f,-4.386162385e-01f,-8.986744657e-01f,-8.193475201e-01f,-5.732971667e-01f,
-4.496113297e-01f,-8.932243012e-01f,-8.280450453e-01f,-5.606615762e-01f,-4.605387110e-01f,-8.876396204e-01f,-8.365477272e-01f,-5.478940592e-01f,
-4.713967368e-01f,-8.819212643e-01f,-8.448535652e-01f,-5.349976199e-01f,-4.821837721e-01f,-8.760700942e-01f,-8.529606049e-01f,-5.219752929e-01f,
-4.928981922e-01f,-8.700869911e-01f,-8.608669386e-01f,-5.088301425e-01f,-5.035383837e-01f,-8.639728561e-01f,-8.685707060e-01f,-4.955652618e-01f,
-5.141027442e-01f,-8.577286100e-01f,-8.760700942e-01f,-4.821837721e-01f,-5.245896827e-01f,-8.513551931e-01f,-8.833633387e-01f,-4.686888220e-01f,
-5.349976199e-01f,-8.448535652e-01f,-8.904487232e-01f,-4.550835871e-01f,-5.453249884e-01f,-8.382247056e-01f,-8.973245807e-01f,-4.413712687e-01f,
-5.555702330e-01f,-8.314696123e-01f,-9.039892931e-01f,-4.275550934e-01f,-5.657318108e-01f,-8.245893028e-01f,-9.104412923e-01f,-4.136383122e-01f,
-5.758081914e-01f,-8.175848132e-01f,-9.166790599e-01f,-3.996241998e-01f,-5.857978575e-01f,-8.104571983e-01f,-9.227011283e-01f,-3.855160538e-01f,
-5.956993045e-01f,-8.032075315e-01f,-9.285060805e-01f,-3.713171940e-01f,-6.055110414e-01f,-7.958369046e-01f,-9.340925504e-01f,-3.570309612e-01f,
-6.152315906e-01f,-7.883464276e-01f,-9.394592236e-01f,-3.426607173e-01f,-6.248594881e-01f,-7.807372286e-01f,-9.446048373e-01f,-3.282098436e-01f,
-6.343932842e-01f,-7.730104534e-01f,-9.495281806e-01f,-3.136817404e-01f,-6.438315429e-01f,-7.651672656e-01f,-9.542280951e-01f,-2.990798263e-01f,
-6.531728430e-01f,-7.572088465e-01f,-9.587034749e-01f,-2.844075372e-01f,-6.624157776e-01f,-7.491363945e-01f,-9.629532669e-01f,-2.696683256e-01f,
-6.715589548e-01f,-7.409511254e-01f,-9.669764710e-01f,-2.548656596e-01f,-6.806009978e-01f,-7.326542717e-01f,-9.707721407e-01f,-2.400030224e-01f,
-6.895405447e-01f,-7.242470830e-01f,-9.743393828e-01f,-2.250839114e-01f,-6.983762494e-01f,-7.157308253e-01f,-9.776773578e-01f,-2.101118369e-01f,
-7.071067812e-01f,-7.071067812e-01f,-9.807852804e-01f,-1.950903220e-01f,-7.157308253e-01f,-6.983762494e-01f,-9.836624192e-01f,-1.800229014e-01f,
-7.242470830e-01f,-6.895405447e-01f,-9.863080972e-01f,-1.649131205e-01f,-7.326542717e-01f,-6.806009978e-01f,-9.887216920e-01f,-1.497645347e-01f,
-7.409511254e-01f,-6.715589548e-01f,-9.909026354e-01f,-1.345807085e-01f,-7.491363945e-01f,-6.624157776e-01f,-9.928504145e-01f,-1.193652148e-01f,
-7.572088465e-01f,-6.531728430e-01f,-9.945645707e-01f,-1.041216339e-01f,-7.651672656e-01f,-6.438315429e-01f,-9.960447009e-01f,-8.885355258e-02f,
-7.730104534e-01f,-6.343932842e-01f,-9.972904567e-01f,-7.356456360e-02f,-7.807372286e-01f,-6.248594881e-01f,-9.983015449e-01f,-5.825826450e-02f,
-7.883464276e-01f,-6.152315906e-01f,-9.990777278e-01f,-4.293825693e-02f,-7.958369046e-01f,-6.055110414e-01f,-9.996188225e-01f,-2.760814578e-02f,
-8.032075315e-01f,-5.956993045e-01f,-9.999247018e-01f,-1.227153829e-02f,-8.104571983e-01f,-5.857978575e-01f,-9.999952938e-01f,3.067956763e-03f,
-8.175848132e-01f,-5.758081914e-01f,-9.998305818e-01f,1.840672991e-02f,-8.245893028e-01f,-5.657318108e-01f,-9.994306046e-01f,3.374117185e-02f,
-8.314696123e-01f,-5.555702330e-01f,-9.987954562e-01f,4.906767433e-02f,-8.382247056e-01f,-5.453249884e-01f,-9.979252862e-01f,6.438263093e-02f,
-8.448535652e-01f,-5.349976199e-01f,-9.968202993e-01f,7.968243797e-02f,-8.513551931e-01f,-5.245896827e-01f,-9.954807555e-01f,9.496349533e-02f,
-8.577286100e-01f,-5.141027442e-01f,-9.939069700e-01f,1.102222073e-01f,-8.639728561e-01f,-5.035383837e-01f,-9.920993131e-01f,1.254549834e-01f,
-8.700869911e-01f,-4.928981922e-01f,-9.900582103e-01f,1.406582393e-01f,-8.760700942e-01f,-4.821837721e-01f,-9.877841416e-01f,1.558283977e-01f,
-8.819212643e-01f,-4.713967368e-01f,-9.852776424e-01f,1.709618888e-01f,-8.876396204e-01f,-4.605387110e-01f,-9.825393023e-01f,1.860551517e-01f,
-8.932243012e-01f,-4.496113297e-01f,-9.795697657e-01f,2.011046348e-01f,-8.986744657e-01f,-4.386162385e-01f,-9.763697313e-01f,2.161067971e-01f,
-9.039892931e-01f,-4.275550934e-01f,-9.729399522e-01f,2.310581083e-01f,-9.091679831e-01f,-4.164295601e-01f,-9.692812354e-01f,2.459550503e-01f,
-9.142097557e-01f,-4.052413140e-01f,-9.653944417e-01f,2.607941179e-01f,-9.191138517e-01f,-3.939920401e-01f,-9.612804858e-01f,2.755718193e-01f,
-9.238795325e-01f,-3.826834324e-01f,-9.569403357e-01f,2.902846773e-01f,-9.285060805e-01f,-3.713171940e-01f,-9.523750127e-01f,3.049292297e-01f,
-9.329927988e-01f,-3.598950365e-01f,-9.475855910e-01f,3.195020308e-01f,-9.373390119e-01f,-3.484186802e-01f,-9.425731976e-01f,3.339996514e-01f,
-9.415440652e-01f,-3.368898534e-01f,-9.373390119e-01f,3.484186802e-01f,-9.456073254e-01f,-3.253102922e-01f,-9.318842656e-01f,3.627557244e-01f,
-9.495281806e-01f,-3.136817404e-01f,-9.262102421e-01f,3.770074102e-01f,-9.533060404e-01f,-3.020059493e-01f,-9.203182767e-01f,3.911703843e-01f,
-9.569403357e-01f,-2.902846773e-01f,-9.142097557e-01f,4.052413140e-01f,-9.604305194e-01f,-2.785196894e-01f,-9.078861165e-01f,4.192168884e-01f,
-9.637760658e-01f,-2.667127575e-01f,-9.013488470e-01f,4.330938189e-01f,-9.669764710e-01f,-2.548656596e-01f,-8.945994856e-01f,4.468688402e-01f,
-9.700312532e-01f,-2.429801799e-01f,-8.876396204e-01f,4.605387110e-01f,-9.729399522e-01f,-2.310581083e-01f,-8.804708891e-01f,4.741002147e-01f,
-9.757021300e-01f,-2.191012402e-01f,-8.730949784e-01f,4.875501601e-01f,-9.783173707e-01f,-2.071113762e-01f,-8.655136241e-01f,5.008853826e-01f,
-9.807852804e-01f,-1.950903220e-01f,-8.577286100e-01f,5.141027442e-01f,-9.831054874e-01f,-1.830398880e-01f,-8.497417680e-01f,5.271991348e-01f,
-9.852776424e-01f,-1.709618888e-01f,-8.415549774e-01f,5.401714727e-01f,-9.873014182e-01f,-1.588581433e-01f,-8.331701647e-01f,5.530167056e-01f,
-9.891765100e-01f,-1.467304745e-01f,-8.245893028e-01f,5.657318108e-01f,-9.909026354e-01f,-1.345807085e-01f,-8.158144108e-01f,5.783137964e-01f,
-9.924795346e-01f,-1.224106752e-01f,-8.068475535e-01f,5.907597019e-01f,-9.939069700e-01f,-1.102222073e-01f,-7.976908409e-01f,6.030665985e-01f,
-9.951847267e-01f,-9.801714033e-02f,-7.883464276e-01f,6.152315906e-01f,-9.963126122e-01f,-8.579731234e-02f,-7.788165124e-01f,6.272518155e-01f,
-9.972904567e-01f,-7.356456360e-02f,-7.691033376e-01f,6.391244449e-01f,-9.981181129e-01f,-6.132073630e-02f,-7.592091890e-01f,6.508466850e-01f,
-9.987954562e-01f,-4.906767433e-02f,-7.491363945e-01f,6.624157776e-01f,-9.993223846e-01f,-3.680722294e-02f,-7.388873245e-01f,6.738290004e-01f,
-9.996988187e-01f,-2.454122852e-02f,-7.284643904e-01f,6.850836678e-01f,-9.999247018e-01f,-1.227153829e-02f,-7.178700451e-01f,6.961771315e-01f,
1.000000000e+00f,0.000000000e+00f,1.000000000e+00f,0.000000000e+00f,9.998305818e-01f,-1.840672991e-02f,9.997694054e-01f,-2.147408028e-02f,
9.993223846e-01f,-3.680722294e-02f,9.990777278e-01f,-4.293825693e-02f,9.984755806e-01f,-5.519524435e-02f,9.979252862e-01f,-6.438263093e-02f,
9.972904567e-01f,-7.356456360e-02f,9.963126122e-01f,-8.579731234e-02f,9.957674145e-01f,-9.190895650e-02f,9.942404495e-01f,-1.071724250e-01f,
9.939069700e-01f,-1.102222073e-01f,9.917097537e-01f,-1.284981108e-01f,9.917097537e-01f,-1.284981108e-01f,9.887216920e-01f,-1.497645347e-01f,
9.891765100e-01f,-1.467304745e-01f,9.852776424e-01f,-1.709618888e-01f,9.863080972e-01f,-1.649131205e-01f,9.813791933e-01f,-1.920803970e-01f,
9.831054874e-01f,-1.830398880e-01f,9.770281427e-01f,-2.131103199e-01f,9.795697657e-01f,-2.011046348e-01f,9.722264971e-01f,-2.340419586e-01f,
9.757021300e-01f,-2.191012402e-01f,9.669764710e-01f,-2.548656596e-01f,9.715038910e-01f,-2.370236060e-01f,9.612804858e-01f,-2.755718193e-01f,
9.669764710e-01f,-2.548656596e-01f,9.551411683e-01f,-2.961508882e-01f,9.621214043e-01f,-2.726213554e-01f,9.485613499e-01f,-3.165933756e-01f,
9.569403357e-01f,-2.902846773e-01f,9.415440652e-01f,-3.368898534e-01f,9.514350210e-01f,-3.078496400e-01f,9.340925504e-01f,-3.570309612e-01f,
9.456073254e-01f,-3.253102922e-01f,9.262102421e-01f,-3.770074102e-01f,9.394592236e-01f,-3.426607173e-01f,9.179007756e-01f,-3.968099874e-01f,
9.329927988e-01f,-3.598950365e-01f,9.091679831e-01f,-4.164295601e-01f,9.262102421e-01f,-3.770074102e-01f,9.000158920e-01f,-4.358570799e-01f,
9.191138517e-01f,-3.939920401e-01f,8.904487232e-01f,-4.550835871e-01f,9.117060320e-01f,-4.108431711e-01f,8.804708891e-01f,-4.741002147e-01f,
9.039892931e-01f,-4.275550934e-01f,8.700869911e-01f,-4.928981922e-01f,8.959662498e-01f,-4.441221446e-01f,8.593018184e-01f,-5.114688504e-01f,
8.876396204e-01f,-4.605387110e-01f,8.481203448e-01f,-5.298036247e-01f,8.790122264e-01f,-4.767992301e-01f,8.365477272e-01f,-5.478940592e-01f,
8.700869911e-01f,-4.928981922e-01f,8.245893028e-01f,-5.657318108e-01f,8.608669386e-01f,-5.088301425e-01f,8.122505866e-01f,-5.833086529e-01f,
8.513551931e-01f,-5.245896827e-01f,7.995372691e-01f,-6.006164794e-01f,8.415549774e-01f,-5.401714727e-01f,7.864552136e-01f,-6.176473079e-01f,
8.314696123e-01f,-5.555702330e-01f,7.730104534e-01f,-6.343932842e-01f,8.211025150e-01f,-5.707807459e-01f,7.592091890e-01f,-6.508466850e-01f,
8.104571983e-01f,-5.857978575e-01f,7.450577854e-01f,-6.669999223e-01f,7.995372691e-01f,-6.006164794e-01f,7.305627692e-01f,-6.828455464e-01f,
7.883464276e-01f,-6.152315906e-01f,7.157308253e-01f,-6.983762494e-01f,7.768884657e-01f,-6.296382389e-01f,7.005687939e-01f,-7.135848688e-01f,
7.651672656e-01f,-6.438315429e-01f,6.850836678e-01f,-7.284643904e-01f,7.531867990e-01f,-6.578066933e-01f,6.692825883e-01f,-7.430079521e-01f,
7.409511254e-01f,-6.715589548e-01f,6.531728430e-01f,-7.572088465e-01f,7.284643904e-01f,-6.850836678e-01f,6.367618612e-01f,-7.710605243e-01f,
7.157308253e-01f,-6.983762494e-01f,6.200572118e-01f,-7.845565972e-01f,7.027547445e-01f,-7.114321957e-01f,6.030665985e-01f,-7.976908409e-01f,
6.895405447e-01f,-7.242470830e-01f,5.857978575e-01f,-8.104571983e-01f,6.760927036e-01f,-7.368165689e-01f,5.682589527e-01f,-8.228497814e-01f,
6.624157776e-01f,-7.491363945e-01f,5.504579729e-01f,-8.348628750e-01f,6.485144010e-01f,-7.612023855e-01f,5.324031279e-01f,-8.464909388e-01f,
6.343932842e-01f,-7.730104534e-01f,5.141027442e-01f,-8.577286100e-01f,6.200572118e-01f,-7.845565972e-01f,4.955652618e-01f,-8.685707060e-01f,
6.055110414e-01f,-7.958369046e-01f,4.767992301e-01f,-8.790122264e-01f,5.907597019e-01f,-8.068475535e-01f,4.578133036e-01f,-8.890483559e-01f,
5.758081914e-01f,-8.175848132e-01f,4.386162385e-01f,-8.986744657e-01f,5.606615762e-01f,-8.280450453e-01f,4.192168884e-01f,-9.078861165e-01f,
5.453249884e-01f,-8.382247056e-01f,3.996241998e-01f,-9.166790599e-01f,5.298036247e-01f,-8.481203448e-01f,3.798472089e-01f,-9.250492408e-01f,
5.141027442e-01f,-8.577286100e-01f,3.598950365e-01f,-9.329927988e-01f,4.982276670e-01f,-8.670462455e-01f,3.397768844e-01f,-9.405060706e-01f,
4.821837721e-01f,-8.760700942e-01f,3.195020308e-01f,-9.475855910e-01f,4.659764958e-01f,-8.847970984e-01f,2.990798263e-01f,-9.542280951e-01f,
4.496113297e-01f,-8.932243012e-01f,2.785196894e-01f,-9.604305194e-01f,4.330938189e-01f,-9.013488470e-01f,2.578311022e-01f,-9.661900034e-01f,
4.164295601e-01f,-9.091679831e-01f,2.370236060e-01f,-9.715038910e-01f,3.996241998e-01f,-9.166790599e-01f,2.161067971e-01f,-9.763697313e-01f,
3.826834324e-01f,-9.238795325e-01f,1.950903220e-01f,-9.807852804e-01f,3.656129978e-01f,-9.307669611e-01f,1.739838734e-01f,-9.847485018e-01f,
3.484186802e-01f,-9.373390119e-01f,1.527971853e-01f,-9.882575677e-01f,3.311063058e-01f,-9.435934582e-01f,1.315400287e-01f,-9.913108598e-01f,
3.136817404e-01f,-9.495281806e-01f,1.102222073e-01f,-9.939069700e-01f,2.961508882e-01f,-9.551411683e-01f,8.885355258e-02f,-9.960447009e-01f,
2.785196894e-01f,-9.604305194e-01f,6.744391956e-02f,-9.977230666e-01f,2.607941179e-01f,-9.653944417e-01f,4.600318213e-02f,-9.989412932e-01f,
2.429801799e-01f,-9.700312532e-01f,2.454122852e-02f,-9.996988187e-01f,2.250839114e-01f,-9.743393828e-01f,3.067956763e-03f,-9.999952938e-01f,
2.071113762e-01f,-9.783173707e-01f,-1.840672991e-02f,-9.998305818e-01f,1.890686641e-01f,-9.819638691e-01f,-3.987292759e-02f,-9.992047586e-01f,
1.709618888e-01f,-9.852776424e-01f,-6.132073630e-02f,-9.981181129e-01f,1.527971853e-01f,-9.882575677e-01f,-8.274026455e-02f,-9.965711458e-01f,
1.345807085e-01f,-9.909026354e-01f,-1.041216339e-01f,-9.945645707e-01f,1.163186309e-01f,-9.932119492e-01f,-1.254549834e-01f,-9.920993131e-01f,
9.801714033e-02f,-9.951847267e-01f,-1.467304745e-01f,-9.891765100e-01f,7.968243797e-02f,-9.968202993e-01f,-1.679382950e-01f,-9.857975092e-01f,
6.132073630e-02f,-9.981181129e-01f,-1.890686641e-01f,-9.819638691e-01f,4.293825693e-02f,-9.990777278e-01f,-2.101118369e-01f,-9.776773578e-01f,
2.454122852e-02f,-9.996988187e-01f,-2.310581083e-01f,-9.729399522e-01f,6.135884649e-03f,-9.999811753e-01f,-2.518978182e-01f,-9.677538371e-01f,
-1.227153829e-02f,-9.999247018e-01f,-2.726213554e-01f,-9.621214043e-01f,-3.067480318e-02f,-9.995294175e-01f,-2.932191627e-01f,-9.560452513e-01f,
-4.906767433e-02f,-9.987954562e-01f,-3.136817404e-01f,-9.495281806e-01f,-6.744391956e-02f,-9.977230666e-01f,-3.339996514e-01f,-9.425731976e-01f,
-8.579731234e-02f,-9.963126122e-01f,-3.541635254e-01f,-9.351835099e-01f,-1.041216339e-01f,-9.945645707e-01f,-3.741640630e-01f,-9.273625257e-01f,
-1.224106752e-01f,-9.924795346e-01f,-3.939920401e-01f,-9.191138517e-01f,-1.406582393e-01f,-9.900582103e-01f,-4.136383122e-01f,-9.104412923e-01f,
-1.588581433e-01f,-9.873014182e-01f,-4.330938189e-01f,-9.013488470e-01f,-1.770042204e-01f,-9.842100924e-01f,-4.523495872e-01f,-8.918407094e-01f,
-1.950903220e-01f,-9.807852804e-01f,-4.713967368e-01f,-8.819212643e-01f,-2.131103199e-01f,-9.770281427e-01f,-4.902264833e-01f,-8.715950867e-01f,
-2.310581083e-01f,-9.729399522e-01f,-5.088301425e-01f,-8.608669386e-01f,-2.489276057e-01f,-9.685220943e-01f,-5.271991348e-01f,-8.497417680e-01f,
-2.667127575e-01f,-9.637760658e-01f,-5.453249884e-01f,-8.382247056e-01f,-2.844075372e-01f,-9.587034749e-01f,-5.631993440e-01f,-8.263210628e-01f,
-3.020059493e-01f,-9.533060404e-01f,-5.808139581e-01f,-8.140363297e-01f,-3.195020308e-01f,-9.475855910e-01f,-5.981607070e-01f,-8.013761717e-01f,
-3.368898534e-01f,-9.415440652e-01f,-6.152315906e-01f,-7.883464276e-01f,-3.541635254e-01f,-9.351835099e-01f,-6.320187359e-01f,-7.749531066e-01f,
-3.713171940e-01f,-9.285060805e-01f,-6.485144010e-01f,-7.612023855e-01f,-3.883450467e-01f,-9.215140393e-01f,-6.647109782e-01f,-7.471006060e-01f,
-4.052413140e-01f,-9.142097557e-01f,-6.806009978e-01f,-7.326542717e-01f,-4.220002708e-01f,-9.065957045e-01f,-6.961771315e-01f,-7.178700451e-01f,
-4.386162385e-01f,-8.986744657e-01f,-7.114321957e-01f,-7.027547445e-01f,-4.550835871e-01f,-8.904487232e-01f,-7.263591551e-01f,-6.873153409e-01f,
-4.713967368e-01f,-8.819212643e-01f,-7.409511254e-01f,-6.715589548e-01f,-4.875501601e-01f,-8.730949784e-01f,-7.552013769e-01f,-6.554928530e-01f,
-5.035383837e-01f,-8.639728561e-01f,-7.691033376e-01f,-6.391244449e-01f,-5.193559902e-01f,-8.545579884e-01f,-7.826505962e-01f,-6.224612794e-01f,
-5.349976199e-01f,-8.448535652e-01f,-7.958369046e-01f,-6.055110414e-01f,-5.504579729e-01f,-8.348628750e-01f,-8.086561816e-01f,-5.882815482e-01f,
-5.657318108e-01f,-8.245893028e-01f,-8.211025150e-01f,-5.707807459e-01f,-5.808139581e-01f,-8.140363297e-01f,-8.331701647e-01f,-5.530167056e-01f,
-5.956993045e-01f,-8.032075315e-01f,-8.448535652e-01f,-5.349976199e-01f,-6.103828063e-01f,-7.921065773e-01f,-8.561473284e-01f,-5.167317990e-01f,
-6.248594881e-01f,-7.807372286e-01f,-8.670462455e-01f,-4.982276670e-01f,-6.391244449e-01f,-7.691033376e-01f,-8.775452902e-01f,-4.794937577e-01f,
-6.531728430e-01f,-7.572088465e-01f,-8.876396204e-01f,-4.605387110e-01f,-6.669999223e-01f,-7.450577854e-01f,-8.973245807e-01f,-4.413712687e-01f,
-6.806009978e-01f,-7.326542717e-01f,-9.065957045e-01f,-4.220002708e-01f,-6.939714609e-01f,-7.200025080e-01f,-9.154487161e-01f,-4.024346509e-01f,
-7.071067812e-01f,-7.071067812e-01f,-9.238795325e-01f,-3.826834324e-01f,-7.200025080e-01f,-6.939714609e-01f,-9.318842656e-01f,-3.627557244e-01f,
-7.326542717e-01f,-6.806009978e-01f,-9.394592236e-01f,-3.426607173e-01f,-7.450577854e-01f,-6.669999223e-01f,-9.466009131e-01f,-3.224076788e-01f,
-7.572088465e-01f,-6.531728430e-01f,-9.533060404e-01f,-3.020059493e-01f,-7.691033376e-01f,-6.391244449e-01f,-9.595715131e-01f,-2.814649379e-01f,
-7.807372286e-01f,-6.248594881e-01f,-9.653944417e-01f,-2.607941179e-01f,-7.921065773e-01f,-6.103828063e-01f,-9.707721407e-01f,-2.400030224e-01f,
-8.032075315e-01f,-5.956993045e-01f,-9.757021300e-01f,-2.191012402e-01f,-8.140363297e-01f,-5.808139581e-01f,-9.801821360e-01f,-1.980984107e-01f,
-8.245893028e-01f,-5.657318108e-01f,-9.842100924e-01f,-1.770042204e-01f,-8.348628750e-01f,-5.504579729e-01f,-9.877841416e-01f,-1.558283977e-01f,
-8.448535652e-01f,-5.349976199e-01f,-9.909026354e-01f,-1.345807085e-01f,-8.545579884e-01f,-5.193559902e-01f,-9.935641355e-01f,-1.132709522e-01f,
-8.639728561e-01f,-5.035383837e-01f,-9.957674145e-01f,-9.190895650e-02f,-8.730949784e-01f,-4.875501601e-01f,-9.975114561e-01f,-7.050457339e-02f,
-8.819212643e-01f,-4.713967368e-01f,-9.987954562e-01f,-4.906767433e-02f,-8.904487232e-01f,-4.550835871e-01f,-9.996188225e-01f,-2.760814578e-02f,
-8.986744657e-01f,-4.386162385e-01f,-9.999811753e-01f,-6.135884649e-03f,-9.065957045e-01f,-4.220002708e-01f,-9.998823475e-01f,1.533920628e-02f,
-9.142097557e-01f,-4.052413140e-01f,-9.993223846e-01f,3.680722294e-02f,-9.215140393e-01f,-3.883450467e-01f,-9.983015449e-01f,5.825826450e-02f,
-9.285060805e-01f,-3.713171940e-01f,-9.968202993e-01f,7.968243797e-02f,-9.351835099e-01f,-3.541635254e-01f,-9.948793308e-01f,1.010698628e-01f,
-9.415440652e-01f,-3.368898534e-01f,-9.924795346e-01f,1.224106752e-01f,-9.475855910e-01f,-3.195020308e-01f,-9.896220175e-01f,1.436950332e-01f,
-9.533060404e-01f,-3.020059493e-01f,-9.863080972e-01f,1.649131205e-01f,-9.587034749e-01f,-2.844075372e-01f,-9.825393023e-01f,1.860551517e-01f,
-9.637760658e-01f,-2.667127575e-01f,-9.783173707e-01f,2.071113762e-01f,-9.685220943e-01f,-2.489276057e-01f,-9.736442497e-01f,2.280720832e-01f,
-9.729399522e-01f,-2.310581083e-01f,-9.685220943e-01f,2.489276057e-01f,-9.770281427e-01f,-2.131103199e-01f,-9.629532669e-01f,2.696683256e-01f,
-9.807852804e-01f,-1.950903220e-01f,-9.569403357e-01f,2.902846773e-01f,-9.842100924e-01f,-1.770042204e-01f,-9.504860739e-01f,3.107671527e-01f,
-9.873014182e-01f,-1.588581433e-01f,-9.435934582e-01f,3.311063058e-01f,-9.900582103e-01f,-1.406582393e-01f,-9.362656672e-01f,3.512927561e-01f,
-9.924795346e-01f,-1.224106752e-01f,-9.285060805e-01f,3.713171940e-01f,-9.945645707e-01f,-1.041216339e-01f,-9.203182767e-01f,3.911703843e-01f,
-9.963126122e-01f,-8.579731234e-02f,-9.117060320e-01f,4.108431711e-01f,-9.977230666e-01f,-6.744391956e-02f,-9.026733182e-01f,4.303264813e-01f,
-9.987954562e-01f,-4.906767433e-02f,-8.932243012e-01f,4.496113297e-01f,-9.995294175e-01f,-3.067480318e-02f,-8.833633387e-01f,4.686888220e-01f,
-9.999247018e-01f,-1.227153829e-02f,-8.730949784e-01f,4.875501601e-01f,-9.999811753e-01f,6.135884649e-03f,-8.624239561e-01f,5.061866453e-01f,
-9.996988187e-01f,2.454122852e-02f,-8.513551931e-01f,5.245896827e-01f,-9.990777278e-01f,4.293825693e-02f,-8.398937942e-01f,5.427507849e-01f,
-9.981181129e-01f,6.132073630e-02f,-8.280450453e-01f,5.606615762e-01f,-9.968202993e-01f,7.968243797e-02f,-8.158144108e-01f,5.783137964e-01f,
-9.951847267e-01f,9.801714033e-02f,-8.032075315e-01f,5.956993045e-01f,-9.932119492e-01f,1.163186309e-01f,-7.902302214e-01f,6.128100824e-01f,
-9.909026354e-01f,1.345807085e-01f,-7.768884657e-01f,6.296382389e-01f,-9.882575677e-01f,1.527971853e-01f,-7.631884173e-01f,6.461760130e-01f,
-9.852776424e-01f,1.709618888e-01f,-7.491363945e-01f,6.624157776e-01f,-9.819638691e-01f,1.890686641e-01f,-7.347388781e-01f,6.783500431e-01f,
-9.783173707e-01f,2.071113762e-01f,-7.200025080e-01f,6.939714609e-01f,-9.743393828e-01f,2.250839114e-01f,-7.049340804e-01f,7.092728264e-01f,
-9.700312532e-01f,2.429801799e-01f,-6.895405447e-01f,7.242470830e-01f,-9.653944417e-01f,2.607941179e-01f,-6.738290004e-01f,7.388873245e-01f,
-9.604305194e-01f,2.785196894e-01f,-6.578066933e-01f,7.531867990e-01f,-9.551411683e-01f,2.961508882e-01f,-6.414810128e-01f,7.671389119e-01f,
-9.495281806e-01f,3.136817404e-01f,-6.248594881e-01f,7.807372286e-01f,-9.435934582e-01f,3.311063058e-01f,-6.079497850e-01f,7.939754776e-01f,
-9.373390119e-01f,3.484186802e-01f,-5.907597019e-01f,8.068475535e-01f,-9.307669611e-01f,3.656129978e-01f,-5.732971667e-01f,8.193475201e-01f,
-9.238795325e-01f,3.826834324e-01f,-5.555702330e-01f,8.314696123e-01f,-9.166790599e-01f,3.996241998e-01f,-5.375870763e-01f,8.432082396e-01f,
-9.091679831e-01f,4.164295601e-01f,-5.193559902e-01f,8.545579884e-01f,-9.013488470e-01f,4.330938189e-01f,-5.008853826e-01f,8.655136241e-01f,
-8.932243012e-01f,4.496113297e-01f,-4.821837721e-01f,8.760700942e-01f,-8.847970984e-01f,4.659764958e-01f,-4.632597836e-01f,8.862225301e-01f,
-8.760700942e-01f,4.821837721e-01f,-4.441221446e-01f,8.959662498e-01f,-8.670462455e-01f,4.982276670e-01f,-4.247796812e-01f,9.052967593e-01f,
-8.577286100e-01f,5.141027442e-01f,-4.052413140e-01f,9.142097557e-01f,-8.481203448e-01f,5.298036247e-01f,-3.855160538e-01f,9.227011283e-01f,
-8.382247056e-01f,5.453249884e-01f,-3.656129978e-01f,9.307669611e-01f,-8.280450453e-01f,5.606615762e-01f,-3.455413250e-01f,9.384035341e-01f,
-8.175848132e-01f,5.758081914e-01f,-3.253102922e-01f,9.456073254e-01f,-8.068475535e-01f,5.907597019e-01f,-3.049292297e-01f,9.523750127e-01f,
-7.958369046e-01f,6.055110414e-01f,-2.844075372e-01f,9.587034749e-01f,-7.845565972e-01f,6.200572118e-01f,-2.637546790e-01f,9.645897933e-01f,
-7.730104534e-01f,6.343932842e-01f,-2.429801799e-01f,9.700312532e-01f,-7.612023855e-01f,6.485144010e-01f,-2.220936210e-01f,9.750253451e-01f,
-7.491363945e-01f,6.624157776e-01f,-2.011046348e-01f,9.795697657e-01f,-7.368165689e-01f,6.760927036e-01f,-1.800229014e-01f,9.836624192e-01f,
-7.242470830e-01f,6.895405447e-01f,-1.588581433e-01f,9.873014182e-01f,-7.114321957e-01f,7.027547445e-01f,-1.376201216e-01f,9.904850843e-01f,
-6.983762494e-01f,7.157308253e-01f,-1.163186309e-01f,9.932119492e-01f,-6.850836678e-01f,7.284643904e-01f,-9.496349533e-02f,9.954807555e-01f,
-6.715589548e-01f,7.409511254e-01f,-7.356456360e-02f,9.972904567e-01f,-6.578066933e-01f,7.531867990e-01f,-5.213170468e-02f,9.986402182e-01f,
-6.438315429e-01f,7.651672656e-01f,-3.067480318e-02f,9.995294175e-01f,-6.296382389e-01f,7.768884657e-01f,-9.203754782e-03f,9.999576446e-01f,
-6.152315906e-01f,7.883464276e-01f,1.227153829e-02f,9.999247018e-01f,-6.006164794e-01f,7.995372691e-01f,3.374117185e-02f,9.994306046e-01f,
-5.857978575e-01f,8.104571983e-01f,5.519524435e-02f,9.984755806e-01f,-5.707807459e-01f,8.211025150e-01f,7.662386139e-02f,9.970600703e-01f,
-5.555702330e-01f,8.314696123e-01f,9.801714033e-02f,9.951847267e-01f,-5.401714727e-01f,8.415549774e-01f,1.193652148e-01f,9.928504145e-01f,
-5.245896827e-01f,8.513551931e-01f,1.406582393e-01f,9.900582103e-01f,-5.088301425e-01f,8.608669386e-01f,1.618863938e-01f,9.868094018e-01f,
-4.928981922e-01f,8.700869911e-01f,1.830398880e-01f,9.831054874e-01f,-4.767992301e-01f,8.790122264e-01f,2.041089661e-01f,9.789481753e-01f,
-4.605387110e-01f,8.876396204e-01f,2.250839114e-01f,9.743393828e-01f,-4.441221446e-01f,8.959662498e-01f,2.459550503e-01f,9.692812354e-01f,
-4.275550934e-01f,9.039892931e-01f,2.667127575e-01f,9.637760658e-01f,-4.108431711e-01f,9.117060320e-01f,2.873474595e-01f,9.578264130e-01f,
-3.939920401e-01f,9.191138517e-01f,3.078496400e-01f,9.514350210e-01f,-3.770074102e-01f,9.262102421e-01f,3.282098436e-01f,9.446048373e-01f,
-3.598950365e-01f,9.329927988e-01f,3.484186802e-01f,9.373390119e-01f,-3.426607173e-01f,9.394592236e-01f,3.684668300e-01f,9.296408958e-01f,
-3.253102922e-01f,9.456073254e-01f,3.883450467e-01f,9.215140393e-01f,-3.078496400e-01f,9.514350210e-01f,4.080441629e-01f,9.129621904e-01f,
-2.902846773e-01f,9.569403357e-01f,4.275550934e-01f,9.039892931e-01f,-2.726213554e-01f,9.621214043e-01f,4.468688402e-01f,8.945994856e-01f,
-2.548656596e-01f,9.669764710e-01f,4.659764958e-01f,8.847970984e-01f,-2.370236060e-01f,9.715038910e-01f,4.848692480e-01f,8.745866523e-01f,
-2.191012402e-01f,9.757021300e-01f,5.035383837e-01f,8.639728561e-01f,-2.011046348e-01f,9.795697657e-01f,5.219752929e-01f,8.529606049e-01f,
-1.830398880e-01f,9.831054874e-01f,5.401714727e-01f,8.415549774e-01f,-1.649131205e-01f,9.863080972e-01f,5.581185312e-01f,8.297612338e-01f,
-1.467304745e-01f,9.891765100e-01f,5.758081914e-01f,8.175848132e-01f,-1.284981108e-01f,9.917097537e-01f,5.932322950e-01f,8.050313311e-01f,
-1.102222073e-01f,9.939069700e-01f,6.103828063e-01f,7.921065773e-01f,-9.190895650e-02f,9.957674145e-01f,6.272518155e-01f,7.788165124e-01f,
-7.356456360e-02f,9.972904567e-01f,6.438315429e-01f,7.651672656e-01f,-5.519524435e-02f,9.984755806e-01f,6.601143421e-01f,7.511651319e-01f,
-3.680722294e-02f,9.993223846e-01f,6.760927036e-01f,7.368165689e-01f,-1.840672991e-02f,9.998305818e-01f,6.917592584e-01f,7.221281939e-01f,
};
__device__ __attribute__((aligned(16))) const float TT2[512] = {
1.000000000e+00f,0.000000000e+00f,1.000000000e+00f,0.000000000e+00f,1.000000000e+00f,0.000000000e+00f,9.996988187e-01f,-2.454122852e-02f,
1.000000000e+00f,0.000000000e+00f,9.987954562e-01f,-4.906767433e-02f,1.000000000e+00f,0.000000000e+00f,9.972904567e-01f,-7.356456360e-02f,
1.000000000e+00f,0.000000000e+00f,9.951847267e-01f,-9.801714033e-02f,1.000000000e+00f,0.000000000e+00f,9.924795346e-01f,-1.224106752e-01f,
1.000000000e+00f,0.000000000e+00f,9.891765100e-01f,-1.467304745e-01f,1.000000000e+00f,0.000000000e+00f,9.852776424e-01f,-1.709618888e-01f,
1.000000000e+00f,0.000000000e+00f,9.807852804e-01f,-1.950903220e-01f,1.000000000e+00f,0.000000000e+00f,9.757021300e-01f,-2.191012402e-01f,
1.000000000e+00f,0.000000000e+00f,9.700312532e-01f,-2.429801799e-01f,1.000000000e+00f,0.000000000e+00f,9.637760658e-01f,-2.667127575e-01f,
1.000000000e+00f,0.000000000e+00f,9.569403357e-01f,-2.902846773e-01f,1.000000000e+00f,0.000000000e+00f,9.495281806e-01f,-3.136817404e-01f,
1.000000000e+00f,0.000000000e+00f,9.415440652e-01f,-3.368898534e-01f,1.000000000e+00f,0.000000000e+00f,9.329927988e-01f,-3.598950365e-01f,
1.000000000e+00f,0.000000000e+00f,9.238795325e-01f,-3.826834324e-01f,1.000000000e+00f,0.000000000e+00f,9.142097557e-01f,-4.052413140e-01f,
1.000000000e+00f,0.000000000e+00f,9.039892931e-01f,-4.275550934e-01f,1.000000000e+00f,0.000000000e+00f,8.932243012e-01f,-4.496113297e-01f,
1.000000000e+00f,0.000000000e+00f,8.819212643e-01f,-4.713967368e-01f,1.000000000e+00f,0.000000000e+00f,8.700869911e-01f,-4.928981922e-01f,
1.000000000e+00f,0.000000000e+00f,8.577286100e-01f,-5.141027442e-01f,1.000000000e+00f,0.000000000e+00f,8.448535652e-01f,-5.349976199e-01f,
1.000000000e+00f,0.000000000e+00f,8.314696123e-01f,-5.555702330e-01f,1.000000000e+00f,0.000000000e+00f,8.175848132e-01f,-5.758081914e-01f,
1.000000000e+00f,0.000000000e+00f,8.032075315e-01f,-5.956993045e-01f,1.000000000e+00f,0.000000000e+00f,7.883464276e-01f,-6.152315906e-01f,
1.000000000e+00f,0.000000000e+00f,7.730104534e-01f,-6.343932842e-01f,1.000000000e+00f,0.000000000e+00f,7.572088465e-01f,-6.531728430e-01f,
1.000000000e+00f,0.000000000e+00f,7.409511254e-01f,-6.715589548e-01f,1.000000000e+00f,0.000000000e+00f,7.242470830e-01f,-6.895405447e-01f,
1.000000000e+00f,0.000000000e+00f,1.000000000e+00f,0.000000000e+00f,9.987954562e-01f,-4.906767433e-02f,9.972904567e-01f,-7.356456360e-02f,
9.951847267e-01f,-9.801714033e-02f,9.891765100e-01f,-1.467304745e-01f,9.891765100e-01f,-1.467304745e-01f,9.757021300e-01f,-2.191012402e-01f,
9.807852804e-01f,-1.950903220e-01f,9.569403357e-01f,-2.902846773e-01f,9.700312532e-01f,-2.429801799e-01f,9.329927988e-01f,-3.598950365e-01f,
9.569403357e-01f,-2.902846773e-01f,9.039892931e-01f,-4.275550934e-01f,9.415440652e-01f,-3.368898534e-01f,8.700869911e-01f,-4.928981922e-01f,
9.238795325e-01f,-3.826834324e-01f,8.314696123e-01f,-5.555702330e-01f,9.039892931e-01f,-4.275550934e-01f,7.883464276e-01f,-6.152315906e-01f,
8.819212643e-01f,-4.713967368e-01f,7.409511254e-01f,-6.715589548e-01f,8.577286100e-01f,-5.141027442e-01f,6.895405447e-01f,-7.242470830e-01f,
8.314696123e-01f,-5.555702330e-01f,6.343932842e-01f,-7.730104534e-01f,8.032075315e-01f,-5.956993045e-01f,5.758081914e-01f,-8.175848132e-01f,
7.730104534e-01f,-6.343932842e-01f,5.141027442e-01f,-8.577286100e-01f,7.409511254e-01f,-6.715589548e-01f,4.496113297e-01f,-8.932243012e-01f,
7.071067812e-01f,-7.071067812e-01f,3.826834324e-01f,-9.238795325e-01f,6.715589548e-01f,-7.409511254e-01f,3.136817404e-01f,-9.495281806e-01f,
6.343932842e-01f,-7.730104534e-01f,2.429801799e-01f,-9.700312532e-01f,5.956993045e-01f,-8.032075315e-01f,1.709618888e-01f,-9.852776424e-01f,
5.555702330e-01f,-8.314696123e-01f,9.801714033e-02f,-9.951847267e-01f,5.141027442e-01f,-8.577286100e-01f,2.454122852e-02f,-9.996988187e-01f,
4.713967368e-01f,-8.819212643e-01f,-4.906767433e-02f,-9.987954562e-01f,4.275550934e-01f,-9.039892931e-01f,-1.224106752e-01f,-9.924795346e-01f,
3.826834324e-01f,-9.238795325e-01f,-1.950903220e-01f,-9.807852804e-01f,3.368898534e-01f,-9.415440652e-01f,-2.667127575e-01f,-9.637760658e-01f,
2.902846773e-01f,-9.569403357e-01f,-3.368898534e-01f,-9.415440652e-01f,2.429801799e-01f,-9.700312532e-01f,-4.052413140e-01f,-9.142097557e-01f,
1.950903220e-01f,-9.807852804e-01f,-4.713967368e-01f,-8.819212643e-01f,1.467304745e-01f,-9.891765100e-01f,-5.349976199e-01f,-8.448535652e-01f,
9.801714033e-02f,-9.951847267e-01f,-5.956993045e-01f,-8.032075315e-01f,4.906767433e-02f,-9.987954562e-01f,-6.531728430e-01f,-7.572088465e-01f,
1.000000000e+00f,0.000000000e+00f,1.000000000e+00f,0.000000000e+00f,9.951847267e-01f,-9.801714033e-02f,9.924795346e-01f,-1.224106752e-01f,
9.807852804e-01f,-1.950903220e-01f,9.700312532e-01f,-2.429801799e-01f,9.569403357e-01f,-2.902846773e-01f,9.329927988e-01f,-3.598950365e-01f,
9.238795325e-01f,-3.826834324e-01f,8.819212643e-01f,-4.713967368e-01f,8.819212643e-01f,-4.713967368e-01f,8.175848132e-01f,-5.758081914e-01f,
8.314696123e-01f,-5.555702330e-01f,7.409511254e-01f,-6.715589548e-01f,7.730104534e-01f,-6.343932842e-01f,6.531728430e-01f,-7.572088465e-01f,
7.071067812e-01f,-7.071067812e-01f,5.555702330e-01f,-8.314696123e-01f,6.343932842e-01f,-7.730104534e-01f,4.496113297e-01f,-8.932243012e-01f,
5.555702330e-01f,-8.314696123e-01f,3.368898534e-01f,-9.415440652e-01f,4.713967368e-01f,-8.819212643e-01f,2.191012402e-01f,-9.757021300e-01f,
3.826834324e-01f,-9.238795325e-01f,9.801714033e-02f,-9.951847267e-01f,2.902846773e-01f,-9.569403357e-01f,-2.454122852e-02f,-9.996988187e-01f,
1.950903220e-01f,-9.807852804e-01f,-1.467304745e-01f,-9.891765100e-01f,9.801714033e-02f,-9.951847267e-01f,-2.667127575e-01f,-9.637760658e-01f,
0.000000000e+00f,-1.000000000e+00f,-3.826834324e-01f,-9.238795325e-01f,-9.801714033e-02f,-9.951847267e-01f,-4.928981922e-01f,-8.700869911e-01f,
-1.950903220e-01f,-9.807852804e-01f,-5.956993045e-01f,-8.032075315e-01f,-2.902846773e-01f,-9.569403357e-01f,-6.895405447e-01f,-7.242470830e-01f,
-3.826834324e-01f,-9.238795325e-01f,-7.730104534e-01f,-6.343932842e-01f,-4.713967368e-01f,-8.819212643e-01f,-8.448535652e-01f,-5.349976199e-01f,
-5.555702330e-01f,-8.314696123e-01f,-9.039892931e-01f,-4.275550934e-01f,-6.343932842e-01f,-7.730104534e-01f,-9.495281806e-01f,-3.136817404e-01f,
-7.071067812e-01f,-7.071067812e-01f,-9.807852804e-01f,-1.950903220e-01f,-7.730104534e-01f,-6.343932842e-01f,-9.972904567e-01f,-7.356456360e-02f,
-8.314696123e-01f,-5.555702330e-01f,-9.987954562e-01f,4.906767433e-02f,-8.819212643e-01f,-4.713967368e-01f,-9.852776424e-01f,1.709618888e-01f,
-9.238795325e-01f,-3.826834324e-01f,-9.569403357e-01f,2.902846773e-01f,-9.569403357e-01f,-2.902846773e-01f,-9.142097557e-01f,4.052413140e-01f,
-9.807852804e-01f,-1.950903220e-01f,-8.577286100e-01f,5.141027442e-01f,-9.951847267e-01f,-9.801714033e-02f,-7.883464276e-01f,6.152315906e-01f,
1.000000000e+00f,0.000000000e+00f,1.000000000e+00f,0.000000000e+00f,9.891765100e-01f,-1.467304745e-01f,9.852776424e-01f,-1.709618888e-01f,
9.569403357e-01f,-2.902846773e-01f,9.415440652e-01f,-3.368898534e-01f,9.039892931e-01f,-4.275550934e-01f,8.700869911e-01f,-4.928981922e-01f,
8.314696123e-01f,-5.555702330e-01f,7.730104534e-01f,-6.343932842e-01f,7.409511254e-01f,-6.715589548e-01f,6.531728430e-01f,-7.572088465e-01f,
6.343932842e-01f,-7.730104534e-01f,5.141027442e-01f,-8.577286100e-01f,5.141027442e-01f,-8.577286100e-01f,3.598950365e-01f,-9.329927988e-01f,
3.826834324e-01f,-9.238795325e-01f,1.950903220e-01f,-9.807852804e-01f,2.429801799e-01f,-9.700312532e-01f,2.454122852e-02f,-9.996988187e-01f,
9.801714033e-02f,-9.951847267e-01f,-1.467304745e-01f,-9.891765100e-01f,-4.906767433e-02f,-9.987954562e-01f,-3.136817404e-01f,-9.495281806e-01f,
-1.950903220e-01f,-9.807852804e-01f,-4.713967368e-01f,-8.819212643e-01f,-3.368898534e-01f,-9.415440652e-01f,-6.152315906e-01f,-7.883464276e-01f,
-4.713967368e-01f,-8.819212643e-01f,-7.409511254e-01f,-6.715589548e-01f,-5.956993045e-01f,-8.032075315e-01f,-8.448535652e-01f,-5.349976199e-01f,
-7.071067812e-01f,-7.071067812e-01f,-9.238795325e-01f,-3.826834324e-01f,-8.032075315e-01f,-5.956993045e-01f,-9.757021300e-01f,-2.191012402e-01f,
-8.819212643e-01f,-4.713967368e-01f,-9.987954562e-01f,-4.906767433e-02f,-9.415440652e-01f,-3.368898534e-01f,-9.924795346e-01f,1.224106752e-01f,
-9.807852804e-01f,-1.950903220e-01f,-9.569403357e-01f,2.902846773e-01f,-9.987954562e-01f,-4.906767433e-02f,-8.932243012e-01f,4.496113297e-01f,
-9.951847267e-01f,9.801714033e-02f,-8.032075315e-01f,5.956993045e-01f,-9.700312532e-01f,2.429801799e-01f,-6.895405447e-01f,7.242470830e-01f,
-9.238795325e-01f,3.826834324e-01f,-5.555702330e-01f,8.314696123e-01f,-8.577286100e-01f,5.141027442e-01f,-4.052413140e-01f,9.142097557e-01f,
-7.730104534e-01f,6.343932842e-01f,-2.429801799e-01f,9.700312532e-01f,-6.715589548e-01f,7.409511254e-01f,-7.356456360e-02f,9.972904567e-01f,
-5.555702330e-01f,8.314696123e-01f,9.801714033e-02f,9.951847267e-01f,-4.275550934e-01f,9.039892931e-01f,2.667127575e-01f,9.637760658e-01f,
-2.902846773e-01f,9.569403357e-01f,4.275550934e-01f,9.039892931e-01f,-1.467304745e-01f,9.891765100e-01f,5.758081914e-01f,8.175848132e-01f,
};
__device__ __attribute__((aligned(16))) const float TT3[64] = {
1.000000000e+00f,0.000000000e+00f,1.000000000e+00f,0.000000000e+00f,1.000000000e+00f,0.000000000e+00f,9.807852804e-01f,-1.950903220e-01f,
1.000000000e+00f,0.000000000e+00f,9.238795325e-01f,-3.826834324e-01f,1.000000000e+00f,0.000000000e+00f,8.314696123e-01f,-5.555702330e-01f,
1.000000000e+00f,0.000000000e+00f,1.000000000e+00f,0.000000000e+00f,9.238795325e-01f,-3.826834324e-01f,8.314696123e-01f,-5.555702330e-01f,
7.071067812e-01f,-7.071067812e-01f,3.826834324e-01f,-9.238795325e-01f,3.826834324e-01f,-9.238795325e-01f,-1.950903220e-01f,-9.807852804e-01f,
1.000000000e+00f,0.000000000e+00f,1.000000000e+00f,0.000000000e+00f,7.071067812e-01f,-7.071067812e-01f,5.555702330e-01f,-8.314696123e-01f,
0.000000000e+00f,-1.000000000e+00f,-3.826834324e-01f,-9.238795325e-01f,-7.071067812e-01f,-7.071067812e-01f,-9.807852804e-01f,-1.950903220e-01f,
1.000000000e+00f,0.000000000e+00f,1.000000000e+00f,0.000000000e+00f,3.826834324e-01f,-9.238795325e-01f,1.950903220e-01f,-9.807852804e-01f,
-7.071067812e-01f,-7.071067812e-01f,-9.238795325e-01f,-3.826834324e-01f,-9.238795325e-01f,3.826834324e-01f,-5.555702330e-01f,8.314696123e-01f,
};
__device__ __attribute__((aligned(16))) const float TTA[1024] = {
1.000000000e+00f,0.000000000e+00f,1.000000000e+00f,0.000000000e+00f,9.999405050e-01f,-1.090809149e-02f,9.997620271e-01f,-2.181488503e-02f,
9.997620271e-01f,-2.181488503e-02f,9.990482216e-01f,-4.361938737e-02f,9.994645875e-01f,-3.271908282e-02f,9.978589232e-01f,-6.540312923e-02f,
9.990482216e-01f,-4.361938737e-02f,9.961946981e-01f,-8.715574275e-02f,9.985129789e-01f,-5.451450164e-02f,9.940563382e-01f,-1.088668749e-01f,
9.978589232e-01f,-6.540312923e-02f,9.914448614e-01f,-1.305261922e-01f,9.970861323e-01f,-7.628397450e-02f,9.883615105e-01f,-1.521233862e-01f,
9.961946981e-01f,-8.715574275e-02f,9.848077530e-01f,-1.736481777e-01f,9.951847267e-01f,-9.801714033e-02f,9.807852804e-01f,-1.950903220e-01f,
9.940563382e-01f,-1.088668749e-01f,9.762960071e-01f,-2.164396139e-01f,9.928096670e-01f,-1.197036553e-01f,9.713420698e-01f,-2.376858923e-01f,
9.914448614e-01f,-1.305261922e-01f,9.659258263e-01f,-2.588190451e-01f,9.899620837e-01f,-1.413331978e-01f,9.600498544e-01f,-2.798290140e-01f,
9.883615105e-01f,-1.521233862e-01f,9.537169507e-01f,-3.007057995e-01f,9.866433321e-01f,-1.628954734e-01f,9.469301295e-01f,-3.214394653e-01f,
9.848077530e-01f,-1.736481777e-01f,9.396926208e-01f,-3.420201433e-01f,9.828549917e-01f,-1.843802195e-01f,9.320078693e-01f,-3.624380383e-01f,
9.807852804e-01f,-1.950903220e-01f,9.238795325e-01f,-3.826834324e-01f,9.785988655e-01f,-2.057772107e-01f,9.153114791e-01f,-4.027466899e-01f,
9.762960071e-01f,-2.164396139e-01f,9.063077870e-01f,-4.226182617e-01f,9.738769793e-01f,-2.270762630e-01f,8.968727415e-01f,-4.422886902e-01f,
9.713420698e-01f,-2.376858923e-01f,8.870108332e-01f,-4.617486132e-01f,9.686915804e-01f,-2.482672394e-01f,8.767267557e-01f,-4.809887689e-01f,
9.659258263e-01f,-2.588190451e-01f,8.660254038e-01f,-5.000000000e-01f,9.630451367e-01f,-2.693400540e-01f,8.549118707e-01f,-5.187732582e-01f,
9.600498544e-01f,-2.798290140e-01f,8.433914458e-01f,-5.372996083e-01f,9.569403357e-01f,-2.902846773e-01f,8.314696123e-01f,-5.555702330e-01f,
9.537169507e-01f,-3.007057995e-01f,8.191520443e-01f,-5.735764364e-01f,9.503800830e-01f,-3.110911408e-01f,8.064446043e-01f,-5.913096484e-01f,
9.469301295e-01f,-3.214394653e-01f,7.933533403e-01f,-6.087614290e-01f,9.433675008e-01f,-3.317495418e-01f,7.798844831e-01f,-6.259234722e-01f,
9.396926208e-01f,-3.420201433e-01f,7.660444431e-01f,-6.427876097e-01f,9.359059268e-01f,-3.522500479e-01f,7.518398075e-01f,-6.593458151e-01f,
9.320078693e-01f,-3.624380383e-01f,7.372773368e-01f,-6.755902076e-01f,9.279989122e-01f,-3.725829021e-01f,7.223639621e-01f,-6.915130558e-01f,
9.238795325e-01f,-3.826834324e-01f,7.071067812e-01f,-7.071067812e-01f,9.196502204e-01f,-3.927384271e-01f,6.915130558e-01f,-7.223639621e-01f,
9.153114791e-01f,-4.027466899e-01f,6.755902076e-01f,-7.372773368e-01f,9.108638249e-01f,-4.127070298e-01f,6.593458151e-01f,-7.518398075e-01f,
9.063077870e-01f,-4.226182617e-01f,6.427876097e-01f,-7.660444431e-01f,9.016439076e-01f,-4.324792063e-01f,6.259234722e-01f,-7.798844831e-01f,
8.968727415e-01f,-4.422886902e-01f,6.087614290e-01f,-7.933533403e-01f,8.919948566e-01f,-4.520455462e-01f,5.913096484e-01f,-8.064446043e-01f,
8.870108332e-01f,-4.617486132e-01f,5.735764364e-01f,-8.191520443e-01f,8.819212643e-01f,-4.713967368e-01f,5.555702330e-01f,-8.314696123e-01f,
8.767267557e-01f,-4.809887689e-01f,5.372996083e-01f,-8.433914458e-01f,8.714279254e-01f,-4.905235682e-01f,5.187732582e-01f,-8.549118707e-01f,
8.660254038e-01f,-5.000000000e-01f,5.000000000e-01f,-8.660254038e-01f,8.605198339e-01f,-5.094169368e-01f,4.809887689e-01f,-8.767267557e-01f,
8.549118707e-01f,-5.187732582e-01f,4.617486132e-01f,-8.870108332e-01f,8.492021815e-01f,-5.280678507e-01f,4.422886902e-01f,-8.968727415e-01f,
8.433914458e-01f,-5.372996083e-01f,4.226182617e-01f,-9.063077870e-01f,8.374803550e-01f,-5.464674328e-01f,4.027466899e-01f,-9.153114791e-01f,
8.314696123e-01f,-5.555702330e-01f,3.826834324e-01f,-9.238795325e-01f,8.253599331e-01f,-5.646069260e-01f,3.624380383e-01f,-9.320078693e-01f,
8.191520443e-01f,-5.735764364e-01f,3.420201433e-01f,-9.396926208e-01f,8.128466846e-01f,-5.824776969e-01f,3.214394653e-01f,-9.469301295e-01f,
8.064446043e-01f,-5.913096484e-01f,3.007057995e-01f,-9.537169507e-01f,7.999465651e-01f,-6.000712399e-01f,2.798290140e-01f,-9.600498544e-01f,
7.933533403e-01f,-6.087614290e-01f,2.588190451e-01f,-9.659258263e-01f,7.866657144e-01f,-6.173791816e-01f,2.376858923e-01f,-9.713420698e-01f,
7.798844831e-01f,-6.259234722e-01f,2.164396139e-01f,-9.762960071e-01f,7.730104534e-01f,-6.343932842e-01f,1.950903220e-01f,-9.807852804e-01f,
1.000000000e+00f,0.000000000e+00f,1.000000000e+00f,0.000000000e+00f,9.994645875e-01f,-3.271908282e-02f,9.990482216e-01f,-4.361938737e-02f,
9.978589232e-01f,-6.540312923e-02f,9.961946981e-01f,-8.715574275e-02f,9.951847267e-01f,-9.801714033e-02f,9.914448614e-01f,-1.305261922e-01f,
9.914448614e-01f,-1.305261922e-01f,9.848077530e-01f,-1.736481777e-01f,9.866433321e-01f,-1.628954734e-01f,9.762960071e-01f,-2.164396139e-01f,
9.807852804e-01f,-1.950903220e-01f,9.659258263e-01f,-2.588190451e-01f,9.738769793e-01f,-2.270762630e-01f,9.537169507e-01f,-3.007057995e-01f,
9.659258263e-01f,-2.588190451e-01f,9.396926208e-01f,-3.420201433e-01f,9.569403357e-01f,-2.902846773e-01f,9.238795325e-01f,-3.826834324e-01f,
9.469301295e-01f,-3.214394653e-01f,9.063077870e-01f,-4.226182617e-01f,9.359059268e-01f,-3.522500479e-01f,8.870108332e-01f,-4.617486132e-01f,
9.238795325e-01f,-3.826834324e-01f,8.660254038e-01f,-5.000000000e-01f,9.108638249e-01f,-4.127070298e-01f,8.433914458e-01f,-5.372996083e-01f,
8.968727415e-01f,-4.422886902e-01f,8.191520443e-01f,-5.735764364e-01f,8.819212643e-01f,-4.713967368e-01f,7.933533403e-01f,-6.087614290e-01f,
8.660254038e-01f,-5.000000000e-01f,7.660444431e-01f,-6.427876097e-01f,8.492021815e-01f,-5.280678507e-01f,7.372773368e-01f,-6.755902076e-01f,
8.314696123e-01f,-5.555702330e-01f,7.071067812e-01f,-7.071067812e-01f,8.128466846e-01f,-5.824776969e-01f,6.755902076e-01f,-7.372773368e-01f,
7.933533403e-01f,-6.087614290e-01f,6.427876097e-01f,-7.660444431e-01f,7.730104534e-01f,-6.343932842e-01f,6.087614290e-01f,-7.933533403e-01f,
7.518398075e-01f,-6.593458151e-01f,5.735764364e-01f,-8.191520443e-01f,7.298640727e-01f,-6.835923020e-01f,5.372996083e-01f,-8.433914458e-01f,
7.071067812e-01f,-7.071067812e-01f,5.000000000e-01f,-8.660254038e-01f,6.835923020e-01f,-7.298640727e-01f,4.617486132e-01f,-8.870108332e-01f,
6.593458151e-01f,-7.518398075e-01f,4.226182617e-01f,-9.063077870e-01f,6.343932842e-01f,-7.730104534e-01f,3.826834324e-01f,-9.238795325e-01f,
6.087614290e-01f,-7.933533403e-01f,3.420201433e-01f,-9.396926208e-01f,5.824776969e-01f,-8.128466846e-01f,3.007057995e-01f,-9.537169507e-01f,
5.555702330e-01f,-8.314696123e-01f,2.588190451e-01f,-9.659258263e-01f,5.280678507e-01f,-8.492021815e-01f,2.164396139e-01f,-9.762960071e-01f,
5.000000000e-01f,-8.660254038e-01f,1.736481777e-01f,-9.848077530e-01f,4.713967368e-01f,-8.819212643e-01f,1.305261922e-01f,-9.914448614e-01f,
4.422886902e-01f,-8.968727415e-01f,8.715574275e-02f,-9.961946981e-01f,4.127070298e-01f,-9.108638249e-01f,4.361938737e-02f,-9.990482216e-01f,
3.826834324e-01f,-9.238795325e-01f,0.000000000e+00f,-1.000000000e+00f,3.522500479e-01f,-9.359059268e-01f,-4.361938737e-02f,-9.990482216e-01f,
3.214394653e-01f,-9.469301295e-01f,-8.715574275e-02f,-9.961946981e-01f,2.902846773e-01f,-9.569403357e-01f,-1.305261922e-01f,-9.914448614e-01f,
2.588190451e-01f,-9.659258263e-01f,-1.736481777e-01f,-9.848077530e-01f,2.270762630e-01f,-9.738769793e-01f,-2.164396139e-01f,-9.762960071e-01f,
1.950903220e-01f,-9.807852804e-01f,-2.588190451e-01f,-9.659258263e-01f,1.628954734e-01f,-9.866433321e-01f,-3.007057995e-01f,-9.537169507e-01f,
1.305261922e-01f,-9.914448614e-01f,-3.420201433e-01f,-9.396926208e-01f,9.801714033e-02f,-9.951847267e-01f,-3.826834324e-01f,-9.238795325e-01f,
6.540312923e-02f,-9.978589232e-01f,-4.226182617e-01f,-9.063077870e-01f,3.271908282e-02f,-9.994645875e-01f,-4.617486132e-01f,-8.870108332e-01f,
0.000000000e+00f,-1.000000000e+00f,-5.000000000e-01f,-8.660254038e-01f,-3.271908282e-02f,-9.994645875e-01f,-5.372996083e-01f,-8.433914458e-01f,
-6.540312923e-02f,-9.978589232e-01f,-5.735764364e-01f,-8.191520443e-01f,-9.801714033e-02f,-9.951847267e-01f,-6.087614290e-01f,-7.933533403e-01f,
-1.305261922e-01f,-9.914448614e-01f,-6.427876097e-01f,-7.660444431e-01f,-1.628954734e-01f,-9.866433321e-01f,-6.755902076e-01f,-7.372773368e-01f,
-1.950903220e-01f,-9.807852804e-01f,-7.071067812e-01f,-7.071067812e-01f,-2.270762630e-01f,-9.738769793e-01f,-7.372773368e-01f,-6.755902076e-01f,
-2.588190451e-01f,-9.659258263e-01f,-7.660444431e-01f,-6.427876097e-01f,-2.902846773e-01f,-9.569403357e-01f,-7.933533403e-01f,-6.087614290e-01f,
-3.214394653e-01f,-9.469301295e-01f,-8.191520443e-01f,-5.735764364e-01f,-3.522500479e-01f,-9.359059268e-01f,-8.433914458e-01f,-5.372996083e-01f,
-3.826834324e-01f,-9.238795325e-01f,-8.660254038e-01f,-5.000000000e-01f,-4.127070298e-01f,-9.108638249e-01f,-8.870108332e-01f,-4.617486132e-01f,
-4.422886902e-01f,-8.968727415e-01f,-9.063077870e-01f,-4.226182617e-01f,-4.713967368e-01f,-8.819212643e-01f,-9.238795325e-01f,-3.826834324e-01f,
1.000000000e+00f,0.000000000e+00f,1.000000000e+00f,0.000000000e+00f,9.985129789e-01f,-5.451450164e-02f,9.978589232e-01f,-6.540312923e-02f,
9.940563382e-01f,-1.088668749e-01f,9.914448614e-01f,-1.305261922e-01f,9.866433321e-01f,-1.628954734e-01f,9.807852804e-01f,-1.950903220e-01f,
9.762960071e-01f,-2.164396139e-01f,9.659258263e-01f,-2.588190451e-01f,9.630451367e-01f,-2.693400540e-01f,9.469301295e-01f,-3.214394653e-01f,
9.469301295e-01f,-3.214394653e-01f,9.238795325e-01f,-3.826834324e-01f,9.279989122e-01f,-3.725829021e-01f,8.968727415e-01f,-4.422886902e-01f,
9.063077870e-01f,-4.226182617e-01f,8.660254038e-01f,-5.000000000e-01f,8.819212643e-01f,-4.713967368e-01f,8.314696123e-01f,-5.555702330e-01f,
8.549118707e-01f,-5.187732582e-01f,7.933533403e-01f,-6.087614290e-01f,8.253599331e-01f,-5.646069260e-01f,7.518398075e-01f,-6.593458151e-01f,
7.933533403e-01f,-6.087614290e-01f,7.071067812e-01f,-7.071067812e-01f,7.589872812e-01f,-6.511054499e-01f,6.593458151e-01f,-7.518398075e-01f,
7.223639621e-01f,-6.915130558e-01f,6.087614290e-01f,-7.933533403e-01f,6.835923020e-01f,-7.298640727e-01f,5.555702330e-01f,-8.314696123e-01f,
6.427876097e-01f,-7.660444431e-01f,5.000000000e-01f,-8.660254038e-01f,6.000712399e-01f,-7.999465651e-01f,4.422886902e-01f,-8.968727415e-01f,
5.555702330e-01f,-8.314696123e-01f,3.826834324e-01f,-9.238795325e-01f,5.094169368e-01f,-8.605198339e-01f,3.214394653e-01f,-9.469301295e-01f,
4.617486132e-01f,-8.870108332e-01f,2.588190451e-01f,-9.659258263e-01f,4.127070298e-01f,-9.108638249e-01f,1.950903220e-01f,-9.807852804e-01f,
3.624380383e-01f,-9.320078693e-01f,1.305261922e-01f,-9.914448614e-01f,3.110911408e-01f,-9.503800830e-01f,6.540312923e-02f,-9.978589232e-01f,
2.588190451e-01f,-9.659258263e-01f,0.000000000e+00f,-1.000000000e+00f,2.057772107e-01f,-9.785988655e-01f,-6.540312923e-02f,-9.978589232e-01f,
1.521233862e-01f,-9.883615105e-01f,-1.305261922e-01f,-9.914448614e-01f,9.801714033e-02f,-9.951847267e-01f,-1.950903220e-01f,-9.807852804e-01f,
4.361938737e-02f,-9.990482216e-01f,-2.588190451e-01f,-9.659258263e-01f,-1.090809149e-02f,-9.999405050e-01f,-3.214394653e-01f,-9.469301295e-01f,
-6.540312923e-02f,-9.978589232e-01f,-3.826834324e-01f,-9.238795325e-01f,-1.197036553e-01f,-9.928096670e-01f,-4.422886902e-01f,-8.968727415e-01f,
-1.736481777e-01f,-9.848077530e-01f,-5.000000000e-01f,-8.660254038e-01f,-2.270762630e-01f,-9.738769793e-01f,-5.555702330e-01f,-8.314696123e-01f,
-2.798290140e-01f,-9.600498544e-01f,-6.087614290e-01f,-7.933533403e-01f,-3.317495418e-01f,-9.433675008e-01f,-6.593458151e-01f,-7.518398075e-01f,
-3.826834324e-01f,-9.238795325e-01f,-7.071067812e-01f,-7.071067812e-01f,-4.324792063e-01f,-9.016439076e-01f,-7.518398075e-01f,-6.593458151e-01f,
-4.809887689e-01f,-8.767267557e-01f,-7.933533403e-01f,-6.087614290e-01f,-5.280678507e-01f,-8.492021815e-01f,-8.314696123e-01f,-5.555702330e-01f,
-5.735764364e-01f,-8.191520443e-01f,-8.660254038e-01f,-5.000000000e-01f,-6.173791816e-01f,-7.866657144e-01f,-8.968727415e-01f,-4.422886902e-01f,
-6.593458151e-01f,-7.518398075e-01f,-9.238795325e-01f,-3.826834324e-01f,-6.993515264e-01f,-7.147778973e-01f,-9.469301295e-01f,-3.214394653e-01f,
-7.372773368e-01f,-6.755902076e-01f,-9.659258263e-01f,-2.588190451e-01f,-7.730104534e-01f,-6.343932842e-01f,-9.807852804e-01f,-1.950903220e-01f,
-8.064446043e-01f,-5.913096484e-01f,-9.914448614e-01f,-1.305261922e-01f,-8.374803550e-01f,-5.464674328e-01f,-9.978589232e-01f,-6.540312923e-02f,
-8.660254038e-01f,-5.000000000e-01f,-1.000000000e+00f,0.000000000e+00f,-8.919948566e-01f,-4.520455462e-01f,-9.978589232e-01f,6.540312923e-02f,
-9.153114791e-01f,-4.027466899e-01f,-9.914448614e-01f,1.305261922e-01f,-9.359059268e-01f,-3.522500479e-01f,-9.807852804e-01f,1.950903220e-01f,
-9.537169507e-01f,-3.007057995e-01f,-9.659258263e-01f,2.588190451e-01f,-9.686915804e-01f,-2.482672394e-01f,-9.469301295e-01f,3.214394653e-01f,
-9.807852804e-01f,-1.950903220e-01f,-9.238795325e-01f,3.826834324e-01f,-9.899620837e-01f,-1.413331978e-01f,-8.968727415e-01f,4.422886902e-01f,
-9.961946981e-01f,-8.715574275e-02f,-8.660254038e-01f,5.000000000e-01f,-9.994645875e-01f,-3.271908282e-02f,-8.314696123e-01f,5.555702330e-01f,
-9.997620271e-01f,2.181488503e-02f,-7.933533403e-01f,6.087614290e-01f,-9.970861323e-01f,7.628397450e-02f,-7.518398075e-01f,6.593458151e-01f,
-9.914448614e-01f,1.305261922e-01f,-7.071067812e-01f,7.071067812e-01f,-9.828549917e-01f,1.843802195e-01f,-6.593458151e-01f,7.518398075e-01f,
-9.713420698e-01f,2.376858923e-01f,-6.087614290e-01f,7.933533403e-01f,-9.569403357e-01f,2.902846773e-01f,-5.555702330e-01f,8.314696123e-01f,
1.000000000e+00f,0.000000000e+00f,1.000000000e+00f,0.000000000e+00f,9.970861323e-01f,-7.628397450e-02f,9.961946981e-01f,-8.715574275e-02f,
9.883615105e-01f,-1.521233862e-01f,9.848077530e-01f,-1.736481777e-01f,9.738769793e-01f,-2.270762630e-01f,9.659258263e-01f,-2.588190451e-01f,
9.537169507e-01f,-3.007057995e-01f,9.396926208e-01f,-3.420201433e-01f,9.279989122e-01f,-3.725829021e-01f,9.063077870e-01f,-4.226182617e-01f,
8.968727415e-01f,-4.422886902e-01f,8.660254038e-01f,-5.000000000e-01f,8.605198339e-01f,-5.094169368e-01f,8.191520443e-01f,-5.735764364e-01f,
8.191520443e-01f,-5.735764364e-01f,7.660444431e-01f,-6.427876097e-01f,7.730104534e-01f,-6.343932842e-01f,7.071067812e-01f,-7.071067812e-01f,
7.223639621e-01f,-6.915130558e-01f,6.427876097e-01f,-7.660444431e-01f,6.675077247e-01f,-7.446028723e-01f,5.735764364e-01f,-8.191520443e-01f,
6.087614290e-01f,-7.933533403e-01f,5.000000000e-01f,-8.660254038e-01f,5.464674328e-01f,-8.374803550e-01f,4.226182617e-01f,-9.063077870e-01f,
4.809887689e-01f,-8.767267557e-01f,3.420201433e-01f,-9.396926208e-01f,4.127070298e-01f,-9.108638249e-01f,2.588190451e-01f,-9.659258263e-01f,
3.420201433e-01f,-9.396926208e-01f,1.736481777e-01f,-9.848077530e-01f,2.693400540e-01f,-9.630451367e-01f,8.715574275e-02f,-9.961946981e-01f,
1.950903220e-01f,-9.807852804e-01f,0.000000000e+00f,-1.000000000e+00f,1.197036553e-01f,-9.928096670e-01f,-8.715574275e-02f,-9.961946981e-01f,
4.361938737e-02f,-9.990482216e-01f,-1.736481777e-01f,-9.848077530e-01f,-3.271908282e-02f,-9.994645875e-01f,-2.588190451e-01f,-9.659258263e-01f,
-1.088668749e-01f,-9.940563382e-01f,-3.420201433e-01f,-9.396926208e-01f,-1.843802195e-01f,-9.828549917e-01f,-4.226182617e-01f,-9.063077870e-01f,
-2.588190451e-01f,-9.659258263e-01f,-5.000000000e-01f,-8.660254038e-01f,-3.317495418e-01f,-9.433675008e-01f,-5.735764364e-01f,-8.191520443e-01f,
-4.027466899e-01f,-9.153114791e-01f,-6.427876097e-01f,-7.660444431e-01f,-4.713967368e-01f,-8.819212643e-01f,-7.071067812e-01f,-7.071067812e-01f,
-5.372996083e-01f,-8.433914458e-01f,-7.660444431e-01f,-6.427876097e-01f,-6.000712399e-01f,-7.999465651e-01f,-8.191520443e-01f,-5.735764364e-01f,
-6.593458151e-01f,-7.518398075e-01f,-8.660254038e-01f,-5.000000000e-01f,-7.147778973e-01f,-6.993515264e-01f,-9.063077870e-01f,-4.226182617e-01f,
-7.660444431e-01f,-6.427876097e-01f,-9.396926208e-01f,-3.420201433e-01f,-8.128466846e-01f,-5.824776969e-01f,-9.659258263e-01f,-2.588190451e-01f,
-8.549118707e-01f,-5.187732582e-01f,-9.848077530e-01f,-1.736481777e-01f,-8.919948566e-01f,-4.520455462e-01f,-9.961946981e-01f,-8.715574275e-02f,
-9.238795325e-01f,-3.826834324e-01f,-1.000000000e+00f,0.000000000e+00f,-9.503800830e-01f,-3.110911408e-01f,-9.961946981e-01f,8.715574275e-02f,
-9.713420698e-01f,-2.376858923e-01f,-9.848077530e-01f,1.736481777e-01f,-9.866433321e-01f,-1.628954734e-01f,-9.659258263e-01f,2.588190451e-01f,
-9.961946981e-01f,-8.715574275e-02f,-9.396926208e-01f,3.420201433e-01f,-9.999405050e-01f,-1.090809149e-02f,-9.063077870e-01f,4.226182617e-01f,
-9.978589232e-01f,6.540312923e-02f,-8.660254038e-01f,5.000000000e-01f,-9.899620837e-01f,1.413331978e-01f,-8.191520443e-01f,5.735764364e-01f,
-9.762960071e-01f,2.164396139e-01f,-7.660444431e-01f,6.427876097e-01f,-9.569403357e-01f,2.902846773e-01f,-7.071067812e-01f,7.071067812e-01f,
-9.320078693e-01f,3.624380383e-01f,-6.427876097e-01f,7.660444431e-01f,-9.016439076e-01f,4.324792063e-01f,-5.735764364e-01f,8.191520443e-01f,
-8.660254038e-01f,5.000000000e-01f,-5.000000000e-01f,8.660254038e-01f,-8.253599331e-01f,5.646069260e-01f,-4.226182617e-01f,9.063077870e-01f,
-7.798844831e-01f,6.259234722e-01f,-3.420201433e-01f,9.396926208e-01f,-7.298640727e-01f,6.835923020e-01f,-2.588190451e-01f,9.659258263e-01f,
-6.755902076e-01f,7.372773368e-01f,-1.736481777e-01f,9.848077530e-01f,-6.173791816e-01f,7.866657144e-01f,-8.715574275e-02f,9.961946981e-01f,
-5.555702330e-01f,8.314696123e-01f,0.000000000e+00f,1.000000000e+00f,-4.905235682e-01f,8.714279254e-01f,8.715574275e-02f,9.961946981e-01f,
-4.226182617e-01f,9.063077870e-01f,1.736481777e-01f,9.848077530e-01f,-3.522500479e-01f,9.359059268e-01f,2.588190451e-01f,9.659258263e-01f,
-2.798290140e-01f,9.600498544e-01f,3.420201433e-01f,9.396926208e-01f,-2.057772107e-01f,9.785988655e-01f,4.226182617e-01f,9.063077870e-01f,
-1.305261922e-01f,9.914448614e-01f,5.000000000e-01f,8.660254038e-01f,-5.451450164e-02f,9.985129789e-01f,5.735764364e-01f,8.191520443e-01f,
2.181488503e-02f,9.997620271e-01f,6.427876097e-01f,7.660444431e-01f,9.801714033e-02f,9.951847267e-01f,7.071067812e-01f,7.071067812e-01f,
};
__device__ __attribute__((aligned(16))) const float TTB[128] = {
1.000000000e+00f,0.000000000e+00f,1.000000000e+00f,0.000000000e+00f,1.000000000e+00f,0.000000000e+00f,9.951847267e-01f,-9.801714033e-02f,
1.000000000e+00f,0.000000000e+00f,9.807852804e-01f,-1.950903220e-01f,1.000000000e+00f,0.000000000e+00f,9.569403357e-01f,-2.902846773e-01f,
1.000000000e+00f,0.000000000e+00f,9.238795325e-01f,-3.826834324e-01f,1.000000000e+00f,0.000000000e+00f,8.819212643e-01f,-4.713967368e-01f,
1.000000000e+00f,0.000000000e+00f,8.314696123e-01f,-5.555702330e-01f,1.000000000e+00f,0.000000000e+00f,7.730104534e-01f,-6.343932842e-01f,
1.000000000e+00f,0.000000000e+00f,1.000000000e+00f,0.000000000e+00f,9.807852804e-01f,-1.950903220e-01f,9.569403357e-01f,-2.902846773e-01f,
9.238795325e-01f,-3.826834324e-01f,8.314696123e-01f,-5.555702330e-01f,8.314696123e-01f,-5.555702330e-01f,6.343932842e-01f,-7.730104534e-01f,
7.071067812e-01f,-7.071067812e-01f,3.826834324e-01f,-9.238795325e-01f,5.555702330e-01f,-8.314696123e-01f,9.801714033e-02f,-9.951847267e-01f,
3.826834324e-01f,-9.238795325e-01f,-1.950903220e-01f,-9.807852804e-01f,1.950903220e-01f,-9.807852804e-01f,-4.713967368e-01f,-8.819212643e-01f,
1.000000000e+00f,0.000000000e+00f,1.000000000e+00f,0.000000000e+00f,9.238795325e-01f,-3.826834324e-01f,8.819212643e-01f,-4.713967368e-01f,
7.071067812e-01f,-7.071067812e-01f,5.555702330e-01f,-8.314696123e-01f,3.826834324e-01f,-9.238795325e-01f,9.801714033e-02f,-9.951847267e-01f,
0.000000000e+00f,-1.000000000e+00f,-3.826834324e-01f,-9.238795325e-01f,-3.826834324e-01f,-9.238795325e-01f,-7.730104534e-01f,-6.343932842e-01f,
-7.071067812e-01f,-7.071067812e-01f,-9.807852804e-01f,-1.950903220e-01f,-9.238795325e-01f,-3.826834324e-01f,-9.569403357e-01f,2.902846773e-01f,
1.000000000e+00f,0.000000000e+00f,1.000000000e+00f,0.000000000e+00f,8.314696123e-01f,-5.555702330e-01f,7.730104534e-01f,-6.343932842e-01f,
3.826834324e-01f,-9.238795325e-01f,1.950903220e-01f,-9.807852804e-01f,-1.950903220e-01f,-9.807852804e-01f,-4.713967368e-01f,-8.819212643e-01f,
-7.071067812e-01f,-7.071067812e-01f,-9.238795325e-01f,-3.826834324e-01f,-9.807852804e-01f,-1.950903220e-01f,-9.569403357e-01f,2.902846773e-01f,
-9.238795325e-01f,3.826834324e-01f,-5.555702330e-01f,8.314696123e-01f,-5.555702330e-01f,8.314696123e-01f,9.801714033e-02f,9.951847267e-01f,
};


typedef float c32 __attribute__((ext_vector_type(2)));
typedef float f32x4 __attribute__((ext_vector_type(4)));
typedef short bf16x8 __attribute__((ext_vector_type(8)));
typedef short s16x4 __attribute__((ext_vector_type(4)));
typedef unsigned u32x4 __attribute__((ext_vector_type(4)));
typedef unsigned short u16;
#define GAS __attribute__((address_space(1)))
#define LAS __attribute__((address_space(3)))


#define RLX_AGENT __ATOMIC_RELAXED, __HIP_MEMORY_SCOPE_AGENT
__device__ __forceinline__ void st_sc1(c32* p, c32 v) { __hip_atomic_store((unsigned long long*)p, __builtin_bit_cast(unsigned long long, v), RLX_AGENT); }
__device__ __forceinline__ void st_sc1_x4(void* p, u32x4 v) {
    asm volatile("global_store_dwordx4 %0, %1, off sc1\n\ts_nop 1" :: "v"(p), "v"(v) : "memory");
}
__device__ __forceinline__ void st_sc1_x4_mfma(void* p, u32x4 v) {
    asm volatile("s_nop 15\n\ts_nop 3\n\tglobal_store_dwordx4 %0, %1, off sc1\n\ts_nop 1" :: "v"(p), "v"(v) : "memory");
}
#define PIN() __builtin_amdgcn_sched_barrier(0)
#define WAVE_SYNC() do { asm volatile("" ::: "memory"); __builtin_amdgcn_wave_barrier(); asm volatile("" ::: "memory"); } while (0)
#define LDS_BARRIER() do { asm volatile("s_waitcnt lgkmcnt(0)" ::: "memory"); __builtin_amdgcn_s_barrier(); asm volatile("" ::: "memory"); } while (0)

#define PF1_DELAY 40
#define PF2_DELAY 56
#define PF3_DELAY 64
#define ST16(p, v) st_sc1_x4((void*)(p), v)
#define D_LEN 576
#define DF_LEN 289
#define F_LEN 4608

#define WS_Z     0
#define WS_WT    (WS_Z + 576 * 1024 * 8)
#define WS_H     (WS_WT + 289 * 2048 * 4)
#define WS_L2    (WS_H + 2048 * 576 * 2)
#define WS_KEY   (WS_L2 + 2048 * 576 * 2)
#define WS_SEL   (WS_KEY + 2048 * 4)
#define WS_PART  (WS_SEL + 256)

__device__ __forceinline__ float bf2f(u16 b) { return __uint_as_float(((unsigned)b) << 16); }
__device__ __forceinline__ u16 f2bf(float f) { __bf16 h = (__bf16)f; return __builtin_bit_cast(u16, h); }
__device__ __forceinline__ float bfr(float f) { return bf2f(f2bf(f)); }

__device__ __forceinline__ c32 add_mi(c32 a, c32 b) {
    c32 r; asm("v_pk_add_f32 %0, %1, %2 op_sel:[0,1] op_sel_hi:[1,0] neg_hi:[0,1]" : "=v"(r) : "v"(a), "v"(b)); return r;
}
__device__ __forceinline__ c32 add_pi(c32 a, c32 b) {
    c32 r; asm("v_pk_add_f32 %0, %1, %2 op_sel:[0,1] op_sel_hi:[1,0] neg_lo:[0,1]" : "=v"(r) : "v"(a), "v"(b)); return r;
}
__device__ __forceinline__ c32 fma_mi(c32 d, c32 k, c32 a) {
    c32 r; asm("v_pk_fma_f32 %0, %1, %2, %3 op_sel:[1,0,0] op_sel_hi:[0,1,1] neg_hi:[1,0,0]" : "=v"(r) : "v"(d), "v"(k), "v"(a)); return r;
}
__device__ __forceinline__ c32 fma_pi(c32 d, c32 k, c32 a) {
    c32 r; asm("v_pk_fma_f32 %0, %1, %2, %3 op_sel:[1,0,0] op_sel_hi:[0,1,1] neg_lo:[1,0,0]" : "=v"(r) : "v"(d), "v"(k), "v"(a)); return r;
}
__device__ __forceinline__ c32 cmul(c32 a, c32 w) {
    c32 t, r;
    asm("v_pk_mul_f32 %0, %1, %2 op_sel:[1,1] op_sel_hi:[1,0]" : "=v"(t) : "v"(a), "v"(w));
    asm("v_pk_fma_f32 %0, %1, %2, %3 op_sel_hi:[0,1,1] neg_lo:[0,0,1]" : "=v"(r) : "v"(a), "v"(w), "v"(t));
    return r;
}
__device__ __forceinline__ c32 cmulc(c32 a, c32 w) {
    c32 t, r;
    asm("v_pk_mul_f32 %0, %1, %2 op_sel:[1,1] op_sel_hi:[1,0]" : "=v"(t) : "v"(w), "v"(a));
    asm("v_pk_fma_f32 %0, %1, %2, %3 op_sel_hi:[0,1,1] neg_hi:[0,0,1]" : "=v"(r) : "v"(w), "v"(a), "v"(t));
    return r;
}
template <int INV> __device__ __forceinline__ c32 tw(c32 a, c32 w) { return INV ? cmulc(a, w) : cmul(a, w); }
template <int INV> __device__ __forceinline__ c32 addr(c32 a, c32 b) { return INV ? add_pi(a, b) : add_mi(a, b); }
template <int INV> __device__ __forceinline__ c32 subr(c32 a, c32 b) { return INV ? add_mi(a, b) : add_pi(a, b); }
template <int INV> __device__ __forceinline__ c32 fmar(c32 d, c32 k, c32 a) { return INV ? fma_pi(d, k, a) : fma_mi(d, k, a); }
template <int INV> __device__ __forceinline__ c32 fmsr(c32 d, c32 k, c32 a) { return INV ? fma_mi(d, k, a) : fma_pi(d, k, a); }
template <int INV> __device__ __forceinline__ c32 mul_w83(c32 t) {
    c32 r;
    if (INV) asm("v_pk_add_f32 %0, %1, %1 op_sel:[0,1] op_sel_hi:[0,1] neg_lo:[1,1] neg_hi:[0,1]" : "=v"(r) : "v"(t));
    else     asm("v_pk_add_f32 %0, %1, %1 op_sel:[1,0] op_sel_hi:[0,1] neg_lo:[0,1] neg_hi:[1,1]" : "=v"(r) : "v"(t));
    return r;
}

template <int INV> __device__ __forceinline__ void dft4(c32& c0, c32& c1, c32& c2, c32& c3) {
    c32 d0 = c0 + c2, d2 = c0 - c2, d1 = c1 + c3, e3 = c1 - c3;
    c0 = d0 + d1; c2 = d0 - d1; c1 = addr<INV>(d2, e3); c3 = subr<INV>(d2, e3);
}
template <int INV> __device__ __forceinline__ void dft8(c32 (&a)[8]) {
    const c32 RR = {0.70710678118654752440f, 0.70710678118654752440f};
    c32 b0 = a[0] + a[4], b4 = a[0] - a[4];
    c32 b1 = a[1] + a[5], t5 = a[1] - a[5];
    c32 b2 = a[2] + a[6], t6 = a[2] - a[6];
    c32 b3 = a[3] + a[7], t7 = a[3] - a[7];
    c32 d0 = b0 + b2, d2 = b0 - b2, d1 = b1 + b3, e3 = b1 - b3;
    a[0] = d0 + d1; a[4] = d0 - d1; a[2] = addr<INV>(d2, e3); a[6] = subr<INV>(d2, e3);
    c32 f0 = addr<INV>(b4, t6), f2 = subr<INV>(b4, t6);
    c32 u5 = addr<INV>(t5, t5), u7 = mul_w83<INV>(t7);
    c32 s = u5 + u7, dd = u5 - u7;
    a[1] = f0 + RR * s; a[5] = f0 - RR * s;
    a[3] = fmar<INV>(dd, RR, f2); a[7] = fmsr<INV>(dd, RR, f2);
}
template <int INV> __device__ __forceinline__ void dft3(c32& a, c32& b, c32& c) {
    const c32 S3 = {0.86602540378443864676f, 0.86602540378443864676f};
    const c32 HF = {0.5f, 0.5f};
    c32 t1 = b + c, d = b - c;
    c32 t2 = a - HF * t1;
    a = a + t1; b = fmar<INV>(d, S3, t2); c = fmsr<INV>(d, S3, t2);
}
template <int INV> __device__ __forceinline__ void dft9(c32 (&a)[9]) {
    const c32 w1 = {0.76604444311897803520f, -0.64278760968653932632f};
    const c32 w2 = {0.17364817766693034885f, -0.98480775301220805937f};
    const c32 w4 = {-0.93969262078590838405f, -0.34202014332566873304f};
    dft3<INV>(a[0], a[3], a[6]);
    dft3<INV>(a[1], a[4], a[7]);
    dft3<INV>(a[2], a[5], a[8]);
    a[4] = tw<INV>(a[4], w1); a[7] = tw<INV>(a[7], w2);
    a[5] = tw<INV>(a[5], w2); a[8] = tw<INV>(a[8], w4);
    dft3<INV>(a[0], a[1], a[2]);
    dft3<INV>(a[3], a[4], a[5]);
    dft3<INV>(a[6], a[7], a[8]);
    c32 x1 = a[3], x2 = a[6], x3 = a[1], x5 = a[7], x6 = a[2], x7 = a[5];
    a[1] = x1; a[2] = x2; a[3] = x3; a[5] = x5; a[6] = x6; a[7] = x7;
}

#define DPP_F(x, ctrl, rmask) __builtin_bit_cast(float, __builtin_amdgcn_update_dpp(0, __builtin_bit_cast(int, x), ctrl, rmask, 0xf, true))
__device__ __forceinline__ float wave_sum(float x) {
    x += DPP_F(x, 0x111, 0xf);
    x += DPP_F(x, 0x112, 0xf);
    x += DPP_F(x, 0x114, 0xf);
    x += DPP_F(x, 0x118, 0xf);
    x += DPP_F(x, 0x142, 0xa);
    x += DPP_F(x, 0x143, 0xc);
    return __builtin_bit_cast(float, __builtin_amdgcn_readlane(__builtin_bit_cast(int, x), 63));
}
#define DPP_U(x, ctrl, rmask) (unsigned)__builtin_amdgcn_update_dpp(0, (int)(x), ctrl, rmask, 0xf, true)
__device__ __forceinline__ unsigned umax(unsigned a, unsigned b) { return a > b ? a : b; }
__device__ __forceinline__ unsigned wave_max_u32(unsigned x) {
    x = umax(x, DPP_U(x, 0x111, 0xf));
    x = umax(x, DPP_U(x, 0x112, 0xf));
    x = umax(x, DPP_U(x, 0x114, 0xf));
    x = umax(x, DPP_U(x, 0x118, 0xf));
    x = umax(x, DPP_U(x, 0x142, 0xa));
    x = umax(x, DPP_U(x, 0x143, 0xc));
    return (unsigned)__builtin_amdgcn_readlane((int)x, 63);
}

__device__ __forceinline__ unsigned pk_bf16(c32 v) {
    typedef __bf16 bf16x2_t __attribute__((ext_vector_type(2)));
    return __builtin_bit_cast(unsigned, __builtin_convertvector(v, bf16x2_t));
}
__device__ __forceinline__ c32 unpk_bf16(unsigned u) { c32 r; r.x = __uint_as_float(u << 16); r.y = __uint_as_float(u & 0xffff0000u); return r; }
__device__ __forceinline__ c32 bfr2(c32 v) { return unpk_bf16(pk_bf16(v)); }
__device__ __forceinline__ c32 wave_sum2(c32 v) { c32 r; r.x = wave_sum(v.x); r.y = wave_sum(v.y); return r; }

__device__ __forceinline__ void ln_rows(const c32 (&x)[9], unsigned (&o)[9]) {
    c32 s = x[0];
#pragma unroll
    for (int j = 1; j < 9; ++j) s += x[j];
    const c32 inv = {1.0f / 576.0f, 1.0f / 576.0f};
    const c32 m = wave_sum2(s) * inv;
    c32 q = {0.f, 0.f};
#pragma unroll
    for (int j = 0; j < 9; ++j) { c32 d = x[j] - m; q += d * d; }
    const c32 vb = bfr2(wave_sum2(q) * inv);
    const c32 mb = bfr2(m);
    const c32 eps = {1e-5f, 1e-5f};
    const c32 ve = bfr2(vb + eps);
    c32 sq; sq.x = __builtin_amdgcn_sqrtf(ve.x); sq.y = __builtin_amdgcn_sqrtf(ve.y);
    const c32 den = bfr2(sq);
    c32 r; r.x = __builtin_amdgcn_rcpf(den.x); r.y = __builtin_amdgcn_rcpf(den.y);
    const c32 two = {2.0f, 2.0f};
    r = r * (two - den * r);
#pragma unroll
    for (int j = 0; j < 9; ++j) o[j] = pk_bf16(bfr2(x[j] - mb) * r);
}

#define PA 72
#define RB 648

__global__ __launch_bounds__(320) void k1_rowfft(const u16* __restrict__ x, const u16* __restrict__ wr,
                                                 const u16* __restrict__ wi, c32* __restrict__ Z,
                                                 unsigned* __restrict__ Wt, const u16* __restrict__ wg) {
    __shared__ c32 bufA[36 * PA];
    __shared__ c32 bufB[4 * RB];
    const int tid = threadIdx.x;
    const int b = (int)blockIdx.x;
    if (b >= 384) {
        const int grp = b & 7, sub = (b - 384) >> 3;
        __builtin_amdgcn_s_sleep(PF1_DELAY);
        unsigned acc = 0;
#pragma unroll
        for (int e = 0; e < 5; ++e) {
            int c = e * 320 + tid; c = c < 1296 ? c : 1295;
            const int r = c / 18, cc = c - r * 18;
            const size_t off = (size_t)(72 * sub + r) * F_LEN + grp * 576 + cc * 32;
            acc ^= *(const unsigned*)(wg + off);
        }
        asm volatile("" :: "v"(acc));
        return;
    }
    if (b >= 256) {
        const int p0 = (b - 256) * 16;
        const int c = tid;
        if (c < DF_LEN) {
            unsigned v[16];
#pragma unroll
            for (int e = 0; e < 16; ++e) {
                const int pos = p0 + e;
                const int j = pos >> 8, t = pos & 255;
                const int k = (t >> 5) + 8 * (t & 7) + 64 * (((t >> 3) & 3) + 4 * (j >> 2)) + 512 * (j & 3);
                v[e] = (unsigned)wr[k * DF_LEN + c] | ((unsigned)wi[k * DF_LEN + c] << 16);
            }
            u32x4* dst = (u32x4*)(Wt + c * 2048 + p0);
#pragma unroll
            for (int e = 0; e < 4; ++e) { u32x4 q = {v[4 * e], v[4 * e + 1], v[4 * e + 2], v[4 * e + 3]}; dst[e] = q; }
        }
        return;
    }
    const int g = (b & 7) * 32 + (b >> 3);
    const int wave = tid >> 6, lane = tid & 63;
    f32x4 tb[4];
#pragma unroll
    for (int q = 0; q < 4; ++q) tb[q] = ((const f32x4*)TTB)[q * 8 + (tid & 7)];
    PIN();
    if (wave < 4) {
        const int sp = 4 * g + wave;
        const u16* pa = x + sp * D_LEN + lane;
        const u16* pb = x + (sp + 1024) * D_LEN + lane;
        c32 xv[9];
#pragma unroll
        for (int j = 0; j < 9; ++j) { xv[j].x = bf2f(pa[64 * j]); xv[j].y = bf2f(pb[64 * j]); }
        f32x4 ta[4];
#pragma unroll
        for (int q = 0; q < 4; ++q) ta[q] = ((const f32x4*)TTA)[q * 64 + lane];
        PIN();
        unsigned lo_[9];
        ln_rows(xv, lo_);
        c32 a[9];
#pragma unroll
        for (int j = 0; j < 9; ++j) a[j] = unpk_bf16(lo_[j]);
        dft9<0>(a);
        c32* dst = bufA + wave * 9 * PA + lane;
        dst[0] = a[0];
#pragma unroll
        for (int q = 0; q < 4; ++q) {
            dst[(2 * q + 1) * PA] = cmul(a[2 * q + 1], ta[q].xy);
            dst[(2 * q + 2) * PA] = cmul(a[2 * q + 2], ta[q].zw);
        }
    }
    __syncthreads();
    if (tid < 288) {
        const int cidx = tid >> 3, n3 = tid & 7;
        const int row = cidx / 9, k1 = cidx - 9 * row;
        const c32* src = bufA + cidx * PA + n3;
        c32 a[8];
#pragma unroll
        for (int n2 = 0; n2 < 8; ++n2) a[n2] = src[n2 * 8];
        dft8<0>(a);
        c32* dst = bufB + row * RB + k1 * 9 + n3;
        dst[0] = a[0];
        dst[81] = cmul(a[1], tb[0].zw);
#pragma unroll
        for (int q = 1; q < 4; ++q) {
            dst[(2 * q) * 81] = cmul(a[2 * q], tb[q].xy);
            dst[(2 * q + 1) * 81] = cmul(a[2 * q + 1], tb[q].zw);
        }
    }
    __syncthreads();
    if (tid < 288) {
        const int row = tid & 3, kl = tid >> 2;
        const c32* src = bufB + row * RB + kl * 9;
        c32 a[8];
#pragma unroll
        for (int n3 = 0; n3 < 8; ++n3) a[n3] = src[n3];
        dft8<0>(a);
        c32* dst = Z + kl * 1024 + 4 * g + row;
#pragma unroll
        for (int k3 = 0; k3 < 8; ++k3) dst[72 * 1024 * k3] = a[k3];
    }
}

#define R1 288
__global__ __launch_bounds__(256) void k2_colfft(c32* __restrict__ Z, const unsigned* __restrict__ Wt, const u16* __restrict__ wu) {
    __shared__ c32 buf[8 * R1];
    const int t = threadIdx.x;
    const int kc = (int)blockIdx.x;
    if (kc >= 289) {
        if (kc < 296) return;
        const int grp = kc & 7, sub = (kc - 296) >> 3;
        __builtin_amdgcn_s_sleep(PF2_DELAY);
        unsigned acc = 0;
#pragma unroll
        for (int e = 0; e < 6; ++e) {
            int c = e * 256 + t; c = c < 1296 ? c : 1295;
            const int r = c / 18, cc = c - r * 18;
            acc ^= *(const unsigned*)(wu + (size_t)(72 * sub + r) * F_LEN + grp * 576 + cc * 32);
        }
        asm volatile("" :: "v"(acc));
        return;
    }
    const int kp = (kc == 0) ? 0 : (576 - kc);
    c32* colP = Z + kc * 1024;
    c32* colQ = Z + kp * 1024;
    c32 P[4], Q[4];
#pragma unroll
    for (int j = 0; j < 4; ++j) { P[j] = colP[t + 256 * j]; Q[j] = colQ[t + 256 * j]; }
    f32x4 t1[4], t2[4], t3[4];
#pragma unroll
    for (int q = 0; q < 4; ++q) t1[q] = ((const f32x4*)TT1)[q * 256 + t];
#pragma unroll
    for (int q = 0; q < 4; ++q) t2[q] = ((const f32x4*)TT2)[q * 32 + (t & 31)];
#pragma unroll
    for (int q = 0; q < 4; ++q) t3[q] = ((const f32x4*)TT3)[q * 4 + (t & 3)];
    unsigned wv[8];
#pragma unroll
    for (int j = 0; j < 8; ++j) wv[j] = Wt[kc * 2048 + j * 256 + t];
    PIN();
    asm volatile("" : "+v"(t1[0]), "+v"(t2[0]), "+v"(t3[0]));
    c32 a[8];
#pragma unroll
    for (int j = 0; j < 4; ++j) {
        c32 A, Bv;
        asm("v_pk_add_f32 %0, %1, %2 neg_hi:[0,1]" : "=v"(A) : "v"(P[j]), "v"(Q[j]));
        asm("v_pk_add_f32 %0, %1, %2 op_sel:[1,1] op_sel_hi:[0,0] neg_hi:[1,0]" : "=v"(Bv) : "v"(P[j]), "v"(Q[j]));
        a[j] = A; a[j + 4] = Bv;
    }
    const int hi = t >> 5, lo = t & 31, n4 = t & 3, mid = (t >> 2) & 7;
    c32* const pN = buf + t;
    c32* const pL = buf + hi * R1 + lo;
    c32* const pM = buf + hi * R1 + mid * 36 + n4;
    c32* const pU = buf + hi * R1 + (lo >> 3) * 33 + (lo & 7) * 4;
#define TWQ(T, q) (((q) & 1) ? T[(q) >> 1].zw : T[(q) >> 1].xy)
    dft8<0>(a);
    pN[0] = a[0];
#pragma unroll
    for (int q = 1; q < 8; ++q) pN[q * R1] = cmul(a[q], TWQ(t1, q));
    __syncthreads();
#pragma unroll
    for (int n2 = 0; n2 < 8; ++n2) a[n2] = pL[n2 * 32];
    WAVE_SYNC();
    dft8<0>(a);
    pL[0] = a[0];
#pragma unroll
    for (int q = 1; q < 8; ++q) pL[q * 36] = cmul(a[q], TWQ(t2, q));
    WAVE_SYNC();
#pragma unroll
    for (int n3 = 0; n3 < 8; ++n3) a[n3] = pM[n3 * 4];
    WAVE_SYNC();
    dft8<0>(a);
    pL[0] = a[0];
#pragma unroll
    for (int q = 1; q < 8; ++q) pL[q * 33] = cmul(a[q], TWQ(t3, q));
    WAVE_SYNC();
#pragma unroll
    for (int e = 0; e < 2; ++e)
#pragma unroll
        for (int m = 0; m < 4; ++m) a[e * 4 + m] = pU[e * 132 + m];
    WAVE_SYNC();
    dft4<0>(a[0], a[1], a[2], a[3]);
    dft4<0>(a[4], a[5], a[6], a[7]);
#pragma unroll
    for (int j = 0; j < 8; ++j) {
        c32 w; w.x = __uint_as_float(wv[j] << 16); w.y = __uint_as_float(wv[j] & 0xffff0000u);
        a[j] = cmul(a[j], w);
    }
    dft4<1>(a[0], a[1], a[2], a[3]);
    dft4<1>(a[4], a[5], a[6], a[7]);
#pragma unroll
    for (int e = 0; e < 2; ++e)
#pragma unroll
        for (int m = 0; m < 4; ++m) pU[e * 132 + m] = a[e * 4 + m];
    WAVE_SYNC();
    a[0] = pL[0];
#pragma unroll
    for (int q = 1; q < 8; ++q) a[q] = cmulc(pL[q * 33], TWQ(t3, q));
    WAVE_SYNC();
    dft8<1>(a);
#pragma unroll
    for (int n3 = 0; n3 < 8; ++n3) pM[n3 * 4] = a[n3];
    WAVE_SYNC();
    a[0] = pL[0];
#pragma unroll
    for (int q = 1; q < 8; ++q) a[q] = cmulc(pL[q * 36], TWQ(t2, q));
    WAVE_SYNC();
    dft8<1>(a);
#pragma unroll
    for (int n2 = 0; n2 < 8; ++n2) pL[n2 * 32] = a[n2];
    __syncthreads();
    a[0] = pN[0];
#pragma unroll
    for (int q = 1; q < 8; ++q) a[q] = cmulc(pN[q * R1], TWQ(t1, q));
    dft8<1>(a);
    if (kc == 0 || kc == 288) {
#pragma unroll
        for (int j = 0; j < 4; ++j) { c32 v; v.x = a[j].x; v.y = a[j + 4].x; st_sc1(&colP[t + 256 * j], v); }
    } else {
#pragma unroll
        for (int j = 0; j < 4; ++j) {
            c32 p = add_pi(a[j], a[j + 4]), q;
            asm("v_pk_add_f32 %0, %1, %2 op_sel:[0,1] op_sel_hi:[1,0] neg_hi:[1,0]" : "=v"(q) : "v"(a[j]), "v"(a[j + 4]));
            st_sc1(&colP[t + 256 * j], p);
            st_sc1(&colQ[t + 256 * j], q);
        }
    }
}

__global__ __launch_bounds__(320) void k3_rowifft(const u16* __restrict__ x, const c32* __restrict__ Z,
                                                  const u16* __restrict__ wrt, u16* __restrict__ out,
                                                  u16* __restrict__ Hb, u16* __restrict__ L2b,
                                                  unsigned* __restrict__ keys, const u16* __restrict__ wd) {
    __shared__ __attribute__((aligned(16))) c32 bufA[36 * PA];
    __shared__ __attribute__((aligned(16))) c32 bufB[4 * RB];
    const int tid = threadIdx.x;
    if (blockIdx.x >= 256) {
        const int grp = blockIdx.x & 7, sub = (blockIdx.x - 256) >> 3;
        __builtin_amdgcn_s_sleep(PF3_DELAY);
        unsigned acc = 0;
        const u16* base = wd + (size_t)(grp * 576 + sub * 72) * D_LEN;
#pragma unroll
        for (int e = 0; e < 5; ++e) {
            int c = e * 320 + tid; c = c < 1296 ? c : 1295;
            acc ^= *(const unsigned*)(base + c * 32);
        }
        asm volatile("" :: "v"(acc));
        return;
    }
    const int b = (int)blockIdx.x;
    const int g = (b & 7) * 32 + (b >> 3);
    const int wave = tid >> 6, lane = tid & 63;
    c32 a0[8];
    if (tid < 288) {
        const c32* src = Z + (tid >> 2) * 1024 + 4 * g + (tid & 3);
#pragma unroll
        for (int k3 = 0; k3 < 8; ++k3) a0[k3] = src[72 * 1024 * k3];
    }
    f32x4 tb[4];
#pragma unroll
    for (int q = 0; q < 4; ++q) tb[q] = ((const f32x4*)TTB)[q * 8 + (tid & 7)];
    u16 xa[9], xb[9], wrv[9];
    f32x4 ta[4];
    if (wave < 4) {
        const int sp = 4 * g + wave;
        const u16* pa = x + sp * D_LEN + lane;
        const u16* pb = x + (sp + 1024) * D_LEN + lane;
#pragma unroll
        for (int q = 0; q < 4; ++q) ta[q] = ((const f32x4*)TTA)[q * 64 + lane];
#pragma unroll
        for (int j = 0; j < 9; ++j) { xa[j] = pa[64 * j]; xb[j] = pb[64 * j]; wrv[j] = wrt[lane + 64 * j]; }
    }
    PIN();
    if (tid < 288) {
        dft8<1>(a0);
        c32* dst = bufB + (tid & 3) * RB + (tid >> 2) * 9;
#pragma unroll
        for (int n3 = 0; n3 < 8; ++n3) dst[n3] = a0[n3];
    }
    LDS_BARRIER();
    if (tid < 288) {
        const int cidx = tid >> 3, n3 = tid & 7;
        const int row = cidx / 9, k1 = cidx - 9 * row;
        const c32* src = bufB + row * RB + k1 * 9 + n3;
        c32 a[8];
        a[0] = src[0];
#pragma unroll
        for (int q = 1; q < 8; ++q) a[q] = cmulc(src[q * 81], TWQ(tb, q));
        dft8<1>(a);
        c32* dst = bufA + cidx * PA + n3;
#pragma unroll
        for (int n2 = 0; n2 < 8; ++n2) dst[n2 * 8] = a[n2];
    }
    LDS_BARRIER();
    if (wave < 4) {
        const c32* src = bufA + wave * 9 * PA + lane;
        c32 a[9];
        a[0] = src[0];
#pragma unroll
        for (int q = 0; q < 4; ++q) {
            a[2 * q + 1] = cmulc(src[(2 * q + 1) * PA], ta[q].xy);
            a[2 * q + 2] = cmulc(src[(2 * q + 2) * PA], ta[q].zw);
        }
        dft9<1>(a);
        const c32 scale = {1.0f / (2.0f * 2048.0f * 576.0f), 1.0f / (2.0f * 2048.0f * 576.0f)};
        c32 h[9];
        unsigned hp[9], lp[9];
#pragma unroll
        for (int j = 0; j < 9; ++j) {
            c32 xv; xv.x = bf2f(xa[j]); xv.y = bf2f(xb[j]);
            hp[j] = pk_bf16(xv + bfr2(a[j] * scale));
            h[j] = unpk_bf16(hp[j]);
        }
        ln_rows(h, lp);
        const int sa = 4 * g + wave, sb = sa + 1024;
        u16* sc = (u16*)(bufA + wave * 9 * PA);
        u16* so = (u16*)(bufB + wave * RB);
        WAVE_SYNC();
        c32 racc = {0.f, 0.f};
#pragma unroll
        for (int j = 0; j < 9; ++j) {
            const float w = bf2f(wrv[j]);
            const c32 l = unpk_bf16(lp[j]);
            const c32 ww = {w, w};
            racc += l * ww;
            const unsigned o_ = pk_bf16(h[j] + l);
            sc[lane + 64 * j] = (u16)lp[j];
            sc[576 + lane + 64 * j] = (u16)(lp[j] >> 16);
            sc[1152 + lane + 64 * j] = (u16)hp[j];
            sc[1728 + lane + 64 * j] = (u16)(hp[j] >> 16);
            so[lane + 64 * j] = (u16)o_;
            so[576 + lane + 64 * j] = (u16)(o_ >> 16);
        }
        WAVE_SYNC();
        const float ra = wave_sum(racc.x), rb = wave_sum(racc.y);
        {
            const u32x4 l0 = *(const u32x4*)(sc + lane * 8);
            const u32x4 l1 = *(const u32x4*)(sc + (64 + lane) * 8);
            const u32x4 l2 = *(const u32x4*)(sc + (128 + (lane & 15)) * 8);
            const u32x4 h0 = *(const u32x4*)(sc + 1152 + lane * 8);
            const u32x4 h1 = *(const u32x4*)(sc + 1152 + (64 + lane) * 8);
            const u32x4 h2 = *(const u32x4*)(sc + 1152 + (128 + (lane & 15)) * 8);
            const u32x4 o0 = *(const u32x4*)(so + lane * 8);
            const u32x4 o1 = *(const u32x4*)(so + (64 + lane) * 8);
            const u32x4 o2 = *(const u32x4*)(so + (128 + (lane & 15)) * 8);
            const int e0 = sa * D_LEN + lane * 8;
            const int e1 = (lane < 8) ? (sa * D_LEN + (64 + lane) * 8) : (sb * D_LEN + (lane - 8) * 8);
            const int e2 = sb * D_LEN + (56 + lane) * 8;
            ST16(out + e0, o0); ST16(out + e1, o1);
            ST16(L2b + e0, l0); ST16(L2b + e1, l1);
            ST16(Hb + e0, h0); ST16(Hb + e1, h1);
            if (lane < 16) { ST16(out + e2, o2); ST16(L2b + e2, l2); ST16(Hb + e2, h2); }
        }
        if (lane < 2) {
            const unsigned bits = f2bf(lane == 0 ? ra : rb);
            const unsigned ord = (bits & 0x8000u) ? (~bits & 0xffffu) : (bits | 0x8000u);
            const int s = lane == 0 ? sa : sb;
            keys[s] = (ord << 16) | (unsigned)(2047 - s);
        }
    }
}

#define XS_LD 584
#define L_WG   0
#define L_WU   36864
#define L_WD   73728
#define L_XS   110592
#define L_RED  129280
#define L_ACT  133376
#define L_CAND 134656
#define L_TOTAL 134800

__device__ __forceinline__ bf16x8 tr_frag(const char* base, int rowstride) {
    s16x4 lo = __builtin_amdgcn_ds_read_tr16_b64_v4i16((LAS s16x4*)(base));
    s16x4 hi = __builtin_amdgcn_ds_read_tr16_b64_v4i16((LAS s16x4*)(base + 4 * rowstride));
    bf16x8 r;
    r[0] = lo[0]; r[1] = lo[1]; r[2] = lo[2]; r[3] = lo[3];
    r[4] = hi[0]; r[5] = hi[1]; r[6] = hi[2]; r[7] = hi[3];
    return r;
}
__device__ __forceinline__ unsigned umin_(unsigned a, unsigned b) { return a < b ? a : b; }
#define CE(a, b) do { unsigned hi_ = umax(a, b), lo_ = umin_(a, b); a = hi_; b = lo_; } while (0)

__global__ __launch_bounds__(768) void k4_moe(const unsigned* __restrict__ keys, const u16* __restrict__ L2b,
                                              const u16* __restrict__ wg, const u16* __restrict__ wu,
                                              const u16* __restrict__ wd, float* __restrict__ part,
                                              unsigned* __restrict__ sel) {
    __shared__ __attribute__((aligned(16))) char lds[L_TOTAL];
    const int tid = threadIdx.x, lane = tid & 63;
    const int wave = __builtin_amdgcn_readfirstlane(tid >> 6);
    const int b = blockIdx.x;
    const int tile = (b & 7) * 18 + (b >> 3);
    const int cs = lane & 15, kq = lane >> 4;
    u16* xs = (u16*)(lds + L_XS);
    u16* act = (u16*)(lds + L_ACT);
    if (wave >= 8) {
        const int w4 = wave - 8;
        LDS_BARRIER();
#pragma unroll
        for (int i = 0; i < 9; ++i) {
            const int p = w4 * 9 + i;
            const size_t off = (size_t)(p * 16 + (lane >> 2)) * F_LEN + 32 * tile + (lane & 3) * 8;
            __builtin_amdgcn_global_load_lds((const GAS void*)(wg + off), (LAS void*)(lds + L_WG + p * 1024), 16, 0, 0);
            __builtin_amdgcn_global_load_lds((const GAS void*)(wu + off), (LAS void*)(lds + L_WU + p * 1024), 16, 0, 0);
        }
#pragma unroll
        for (int i = 0; i < 9; ++i) {
            const int p = w4 * 9 + i;
            __builtin_amdgcn_global_load_lds((const GAS void*)(wd + (size_t)tile * 32 * D_LEN + p * 512 + lane * 8),
                                             (LAS void*)(lds + L_WD + p * 1024), 16, 0, 0);
        }
        asm volatile("s_waitcnt vmcnt(0)" ::: "memory");
        if (lane == 0) __hip_atomic_fetch_add((LAS unsigned*)(lds + L_CAND + 128), 1u, __ATOMIC_RELAXED, __HIP_MEMORY_SCOPE_WORKGROUP);
        asm volatile("s_waitcnt lgkmcnt(0)" ::: "memory");
        return;
    }
    {
        u32x4 kv = {0u, 0u, 0u, 0u}, kw = {0u, 0u, 0u, 0u};
        if (wave < 4) { kv = ((const u32x4*)keys)[tid * 2]; kw = ((const u32x4*)keys)[tid * 2 + 1]; }
        if (tid < 2) ((volatile LAS unsigned*)(lds + L_CAND + 128))[tid] = 0u;
        PIN();
        LDS_BARRIER();
        for (int e = tid; e < 8 * 73; e += 512) { u32x4 z = {0u, 0u, 0u, 0u}; *(u32x4*)(xs + 8 * XS_LD + e * 8) = z; }
        if (tid < 40) { u32x4 z = {0u, 0u, 0u, 0u}; *(u32x4*)(act + 8 * 40 + tid * 8) = z; }
        if (wave < 4) {
            unsigned k0 = kv[0], k1 = kv[1], k2 = kv[2], k3 = kv[3], k4 = kw[0], k5 = kw[1], k6 = kw[2], k7 = kw[3];
            CE(k0,k1); CE(k2,k3); CE(k4,k5); CE(k6,k7);
            CE(k0,k2); CE(k1,k3); CE(k4,k6); CE(k5,k7);
            CE(k1,k2); CE(k5,k6); CE(k0,k4); CE(k3,k7);
            CE(k1,k5); CE(k2,k6);
            CE(k1,k4); CE(k3,k6);
            CE(k2,k4); CE(k3,k5);
            CE(k3,k4);
            unsigned mine = 0;
#pragma unroll
            for (int r = 0; r < 8; ++r) {
                const unsigned m = wave_max_u32(k0);
                const bool win = (k0 == m);
                k0 = win ? k1 : k0; k1 = win ? k2 : k1; k2 = win ? k3 : k2; k3 = win ? k4 : k3;
                k4 = win ? k5 : k4; k5 = win ? k6 : k5; k6 = win ? k7 : k6; k7 = win ? 0u : k7;
                mine = (lane == r) ? m : mine;
            }
            if (lane < 8) ((unsigned*)(lds + L_CAND))[wave * 8 + lane] = mine;
        }
    }
    {
        asm volatile("s_waitcnt lgkmcnt(0)" ::: "memory");
        LAS unsigned* c2 = (LAS unsigned*)(lds + L_CAND + 132);
        if (wave < 4 && lane == 0) __hip_atomic_fetch_add(c2, 1u, __ATOMIC_RELAXED, __HIP_MEMORY_SCOPE_WORKGROUP);
        while (__builtin_amdgcn_readfirstlane(__hip_atomic_load(c2, __ATOMIC_RELAXED, __HIP_MEMORY_SCOPE_WORKGROUP)) < 4u) { }
        asm volatile("" ::: "memory");
    }
    {
        const unsigned* cd = (const unsigned*)(lds + L_CAND);
        const unsigned me = cd[lane & 31];
        int rank = 0;
#pragma unroll
        for (int j = 0; j < 8; ++j) {
            const u32x4 q = *(const u32x4*)(cd + 4 * j);
            rank += (q[0] > me) + (q[1] > me) + (q[2] > me) + (q[3] > me);
        }
        const unsigned long long bal = __ballot(rank == wave && lane < 32);
        const int src = __builtin_ctzll(bal | (1ull << 63));
        const unsigned tp = (unsigned)__builtin_amdgcn_readlane((int)me, src);
        if (b == 0 && lane == 0) sel[wave] = tp;
        const int srow = 2047 - (int)(tp & 0x7ffu);
        const u32x4 v0 = *(const u32x4*)(L2b + srow * D_LEN + lane * 8);
        const u32x4 v1 = *(const u32x4*)(L2b + srow * D_LEN + (64 + (lane & 7)) * 8);
        *(u32x4*)(xs + wave * XS_LD + lane * 8) = v0;
        if (lane < 8) *(u32x4*)(xs + wave * XS_LD + (64 + lane) * 8) = v1;
    }
    LDS_BARRIER();
    float* red8 = (float*)(lds + L_RED);
    {
        f32x4 acc = {0.f, 0.f, 0.f, 0.f};
        const int mtx = (wave >> 1) & 1, cb = wave & 1, kh = wave >> 2;
        const char* wbase = lds + (mtx ? L_WU : L_WG) + cb * 32 + (kh * 288 + 8 * kq + ((lane >> 2) & 3)) * 64 + (lane & 3) * 8;
        const u16* abase = xs + cs * XS_LD + kh * 288 + 8 * kq;
#pragma unroll
        for (int i = 0; i < 9; ++i) {
            bf16x8 af = *(const bf16x8*)(abase + i * 32);
            bf16x8 bfrag = tr_frag(wbase + i * 32 * 64, 64);
            acc = __builtin_amdgcn_mfma_f32_16x16x32_bf16(af, bfrag, acc, 0, 0, 0);
        }
        if (lane < 32) *(f32x4*)(red8 + (wave * 32 + lane) * 4) = acc;
    }
    LDS_BARRIER();
    if (wave < 2 && lane < 32) {
        const f32x4 g0 = *(const f32x4*)(red8 + ((0 + wave) * 32 + lane) * 4);
        const f32x4 g1 = *(const f32x4*)(red8 + ((4 + wave) * 32 + lane) * 4);
        const f32x4 u0 = *(const f32x4*)(red8 + ((2 + wave) * 32 + lane) * 4);
        const f32x4 u1 = *(const f32x4*)(red8 + ((6 + wave) * 32 + lane) * 4);
#pragma unroll
        for (int r = 0; r < 4; ++r) {
            const int token = kq * 4 + r;
            const float gp = bfr(g0[r] + g1[r]);
            const float up = bfr(u0[r] + u1[r]);
            const float gg = bfr(gp / (1.0f + __expf(-gp)));
            act[token * 40 + wave * 16 + cs] = f2bf(gg * up);
        }
    }
    LDS_BARRIER();
    {
        bf16x8 af = *(const bf16x8*)(act + cs * 40 + 8 * kq);
        const char* wbase = lds + L_WD + (8 * kq + ((lane >> 2) & 3)) * 1152 + (lane & 3) * 8;
#pragma unroll
        for (int i = 0; i < 5; ++i) {
            const int cb = wave + 8 * i;
            if (cb < 36) {
                bf16x8 bfrag = tr_frag(wbase + cb * 32, 1152);
                f32x4 z = {0.f, 0.f, 0.f, 0.f};
                f32x4 d = __builtin_amdgcn_mfma_f32_16x16x32_bf16(af, bfrag, z, 0, 0, 0);
                if (kq < 2) st_sc1_x4_mfma(part + ((size_t)tile * D_LEN + 16 * cb + cs) * 8 + kq * 4, __builtin_bit_cast(u32x4, d));
            }
        }
    }
}

__global__ __launch_bounds__(256) void k5_final(const float* __restrict__ part, const unsigned* __restrict__ sel,
                                                const u16* __restrict__ Hb, const u16* __restrict__ L2b,
                                                u16* __restrict__ out) {
    __shared__ float red[8][32];
    const int tid = threadIdx.x, o = tid & 31, pg = tid >> 5;
    const int token = o & 7, d = blockIdx.x * 4 + (o >> 3);
    const unsigned key = sel[token];
    float v[18];
#pragma unroll
    for (int p = 0; p < 18; ++p) v[p] = part[((size_t)(pg * 18 + p) * D_LEN + blockIdx.x * 4) * 8 + o];
    const int s_ = 2047 - (int)(key & 0x7ffu);
    const u16 l2b = L2b[s_ * D_LEN + d], hb = Hb[s_ * D_LEN + d];
    float s = 0.f;
#pragma unroll
    for (int p = 0; p < 18; ++p) s += v[p];
    red[pg][o] = s;
    __syncthreads();
    if (tid < 32) {
        float y = 0.f;
#pragma unroll
        for (int p = 0; p < 8; ++p) y += red[p][o];
        y = bfr(y);
        const unsigned ord = key >> 16;
        const unsigned bits = (ord & 0x8000u) ? (ord & 0x7fffu) : (~ord & 0xffffu);
        const float r = bf2f((u16)bits);
        const float sg = bfr(1.0f / (1.0f + __expf(-r)));
        const float ys = bfr(y * sg);
        const float t = bfr(bf2f(l2b) + ys);
        out[s_ * D_LEN + d] = f2bf(bf2f(hb) + t);
    }
}

extern "C" void kernel_launch(void* const* d_in, const int* in_sizes, int n_in,
                              void* d_out, int out_size, void* d_ws, size_t ws_size,
                              hipStream_t stream) {
    const u16* x = (const u16*)d_in[0];
    const u16* wr = (const u16*)d_in[1];
    const u16* wi = (const u16*)d_in[2];
    const u16* wrt = (const u16*)d_in[4];
    const u16* wg = (const u16*)d_in[5];
    const u16* wu = (const u16*)d_in[6];
    const u16* wd = (const u16*)d_in[7];
    u16* out = (u16*)d_out;
    char* ws = (char*)d_ws;
    c32* Z = (c32*)(ws + WS_Z);
    unsigned* Wt = (unsigned*)(ws + WS_WT);
    u16* Hb = (u16*)(ws + WS_H);
    u16* L2b = (u16*)(ws + WS_L2);
    unsigned* keys = (unsigned*)(ws + WS_KEY);
    unsigned* sel = (unsigned*)(ws + WS_SEL);
    float* part = (float*)(ws + WS_PART);
    k1_rowfft<<<64 + 256 + 128, 320, 0, stream>>>(x, wr, wi, Z, Wt, wg);
    k2_colfft<<<296 + 64, 256, 0, stream>>>(Z, Wt, wu);
    k3_rowifft<<<64 + 256, 320, 0, stream>>>(x, Z, wrt, out, Hb, L2b, keys, wd);
    k4_moe<<<144, 768, 0, stream>>>(keys, L2b, wg, wu, wd, part, sel);
    k5_final<<<144, 256, 0, stream>>>(part, sel, Hb, L2b, out);
}
```
